```python
import torch

def _softmax(x, axis=-1):
    return torch.softmax(x, dim=axis)

def _gelu(x):
    return torch.nn.functional.gelu(x, approximate='tanh')

def _silu(x):
    return torch.nn.functional.silu(x)

def _sigmoid(x):
    return torch.sigmoid(x)

def _layernorm(x, axis=-1, eps=1e-05):
    m = x.mean(dim=axis, keepdim=True)
    v = x.var(dim=axis, keepdim=True, unbiased=False)
    return (x - m) / torch.sqrt(v + eps)

def _scatter_add(tgt, idx, src, axis=0):
    return tgt.clone().index_add_(axis, idx, src)

def _segment_sum(src, seg, n):
    out = torch.zeros((n,) + src.shape[1:], dtype=src.dtype)
    return out.index_add_(0, seg, src)

def _pad(x, pw):
    flat = tuple((v for pair in reversed(pw) for v in pair))
    return torch.nn.functional.pad(x, flat)
kernel_name = 'standalone__molecular_angular_painn__graph'

def setup_inputs():

    _w = lambda a: (lambda t: t.to(torch.bfloat16) if t.is_floating_point() else t)(torch.as_tensor(a))
    N, deg, D = (50000, 16, 64)
    n_rbf = max(16, 4)
    E = N * deg
    cutoff = 5.0
    freqs = (torch.arange(1, n_rbf + 1) * torch.pi / cutoff).to(float)
    return {'sv': _w((lambda *s: torch.randn(*s, dtype=torch.bfloat16) * 0.4)(N, 4, D) * 0.3), 'pos': _w((lambda *s: torch.randn(*s, dtype=torch.bfloat16) * 3.2)(N, 3) * 1.2), 'src': _w((lambda lo, hi, *s: torch.randint(lo, hi, s))(0, N, E).to(torch.int64)), 'dst': _w((lambda lo, hi, *s: torch.randint(lo, hi, s))(0, N, E).to(torch.int64)), 'freqs': _w(freqs), 'w_phi1': _w((lambda *s: torch.randn(*s, dtype=torch.bfloat16) * 3.2)(D, D) / D ** 0.5), 'w_phi2': _w((lambda *s: torch.randn(*s, dtype=torch.bfloat16) * 3.2)(D, 3 * D) / D ** 0.5), 'w_rbf': _w((lambda *s: torch.randn(*s, dtype=torch.bfloat16) * 3.2)(n_rbf, 3 * D) / n_rbf ** 0.5)}

def reference(sv, pos, src, dst, freqs, w_phi1, w_phi2, w_rbf):
    N, _, D = sv.shape
    E = src.shape[0]
    PI, cutoff = (3.141592653589793, 5.0)
    s, v = (sv[:, 0, :].clone(), sv[:, 1:, :].clone())
    r_vec = pos[src] - pos[dst]
    d = torch.sqrt((r_vec * r_vec).sum(-1) + 1e-08)
    r_hat = r_vec / d[:, None]
    rbf = torch.sin(d[:, None] * freqs[None, :]) / d[:, None]
    fcut = 0.5 * (torch.cos(PI * d / cutoff) + 1.0)
    fcut = torch.where(d < cutoff, fcut, 0.0 * d)
    Wf = rbf @ w_rbf * fcut[:, None]
    phi = (_silu(s @ w_phi1) @ w_phi2)[src]
    m = phi * Wf
    m_s, m_vv, m_vr = (m[:, :D].clone(), m[:, D:2 * D].clone(), m[:, 2 * D:].clone())
    ds = _segment_sum(m_s, dst, N)
    dv_e = m_vv[:, None, :] * v[src] + m_vr[:, None, :] * r_hat[:, :, None]
    dv = _segment_sum(dv_e.reshape(E, 3 * D), dst, N).reshape(N, 3, D)
    return sv + torch.concatenate([ds[:, None, :], dv], axis=1)
```

```cpp
#include <hip/hip_runtime.h>
#include <hip/hip_bf16.h>
#include <stdint.h>

#define MIMIC 0

__device__ __forceinline__ float bf2f(uint16_t u) { return __uint_as_float(((uint32_t)u) << 16); }
__device__ __forceinline__ float rbf16(float x) {
  return x;
}
__device__ __forceinline__ uint16_t f2bf(float x) { uint32_t u = __float_as_uint(x); u += 0x7fffu + ((u >> 16) & 1u); return (uint16_t)(u >> 16); }

__global__ void k_phi(const uint16_t* sv, const uint16_t* w1, const uint16_t* w2, float* phi, float* acc, int N) {
  __shared__ float s[64], h[64];
  int n = blockIdx.x; int t = threadIdx.x;
  if (t < 64) s[t] = bf2f(sv[(size_t)n * 256 + t]);
  __syncthreads();
  if (t < 64) { float a = 0; for (int i = 0; i < 64; ++i) a += s[i] * bf2f(w1[i * 64 + t]); a = rbf16(a); h[t] = rbf16(a / (1.f + expf(-a))); }
  __syncthreads();
  float a = 0; for (int j = 0; j < 64; ++j) a += h[j] * bf2f(w2[j * 192 + t]);
  phi[(size_t)n * 192 + t] = rbf16(a);
  acc[(size_t)n * 256 + t] = 0.f; if (t < 64) acc[(size_t)n * 256 + 192 + t] = 0.f;
}

__global__ void k_edge(const uint16_t* sv, const uint16_t* pos, const int* src, const int* dst, const uint16_t* freqs,
                       const uint16_t* wr, const float* phi, float* acc, int E) {
  int e = blockIdx.x * 4 + (threadIdx.x >> 6); int t = threadIdx.x & 63;
  if (e >= E) return;
  int a = src[e], b = dst[e];
  float r[3];
  for (int c = 0; c < 3; ++c) r[c] = rbf16(bf2f(pos[a * 3 + c]) - bf2f(pos[b * 3 + c]));
  float ss = rbf16(rbf16(r[0] * r[0]) + rbf16(r[1] * r[1]) + rbf16(r[2] * r[2]));
  float d = rbf16(sqrtf(rbf16(ss + 1e-8f)));
  if (!(d < 5.0f)) return;
  float rh[3]; for (int c = 0; c < 3; ++c) rh[c] = rbf16(r[c] / d);
  float fc = rbf16(0.5f * rbf16(rbf16(cosf(rbf16(rbf16(3.14159265358979f * d) / 5.0f))) + 1.0f));
  float W[3] = {0, 0, 0};
  for (int k = 0; k < 16; ++k) {
    float rb = rbf16(rbf16(sinf(rbf16(d * bf2f(freqs[k])))) / d);
    for (int p = 0; p < 3; ++p) W[p] += rb * bf2f(wr[k * 192 + p * 64 + t]);
  }
  float m[3];
  for (int p = 0; p < 3; ++p) m[p] = rbf16(phi[(size_t)a * 192 + p * 64 + t] * rbf16(rbf16(W[p]) * fc));
  atomicAdd(&acc[(size_t)b * 256 + t], m[0]);
  for (int c = 0; c < 3; ++c) {
    float v = bf2f(sv[(size_t)a * 256 + 64 + c * 64 + t]);
    atomicAdd(&acc[(size_t)b * 256 + 64 + c * 64 + t], rbf16(rbf16(m[1] * v) + rbf16(m[2] * rh[c])));
  }
}

__global__ void k_final(const uint16_t* sv, const float* acc, uint16_t* out, int n) {
  int i = blockIdx.x * 256 + threadIdx.x;
  if (i < n) out[i] = f2bf(bf2f(sv[i]) + rbf16(acc[i]));
}

extern "C" void kernel_launch(void* const* d_in, const int* in_sizes, int n_in,
                              void* d_out, int out_size, void* d_ws, size_t ws_size,
                              hipStream_t stream) {
  const uint16_t* sv = (const uint16_t*)d_in[0];
  const uint16_t* pos = (const uint16_t*)d_in[1];
  const int* src = (const int*)d_in[2];
  const int* dst = (const int*)d_in[3];
  const uint16_t* freqs = (const uint16_t*)d_in[4];
  const uint16_t* w1 = (const uint16_t*)d_in[5];
  const uint16_t* w2 = (const uint16_t*)d_in[6];
  const uint16_t* wr = (const uint16_t*)d_in[7];
  int N = in_sizes[1] / 3, E = in_sizes[2];
  float* phi = (float*)d_ws; float* acc = phi + (size_t)N * 192;
  k_phi<<<N, 192, 0, stream>>>(sv, w1, w2, phi, acc, N);
  k_edge<<<(E + 3) / 4, 256, 0, stream>>>(sv, pos, src, dst, freqs, wr, phi, acc, E);
  k_final<<<(out_size + 255) / 256, 256, 0, stream>>>(sv, acc, (uint16_t*)d_out, out_size);
}
```

```cpp
#include <hip/hip_runtime.h>
#include <hip/hip_bf16.h>
#include <stdint.h>

typedef __attribute__((ext_vector_type(8))) __bf16 bf16x8;
typedef __attribute__((ext_vector_type(2))) __bf16 bf16x2;
typedef __attribute__((ext_vector_type(16))) float f32x16;
typedef __attribute__((ext_vector_type(2))) float f32x2;
typedef __attribute__((ext_vector_type(4))) unsigned u32x4;
typedef __attribute__((ext_vector_type(2))) unsigned u32x2;
typedef __attribute__((ext_vector_type(4))) short s16x4;
#define LAS __attribute__((address_space(3)))
#define DI __device__ __forceinline__

constexpr int NWIN = 7;
constexpr int NPW = 28;
constexpr int NPB = NWIN * NPW;
constexpr int MAXB = 256;
constexpr int MAXP = 176;
constexpr int GRID = 256;
constexpr int NX = GRID - MAXP;
constexpr int ET = 768, KE = 6;
constexpr int EPW = ET * KE;
constexpr int PSL = MAXP / 8;
constexpr int K1T = 1024;
constexpr int K2T = 512;
constexpr int NFRAG = 30;
constexpr unsigned SENT = 0xFFFFFF00u;
constexpr unsigned OOB = 0xFFFFF000u;

constexpr unsigned DB0 = 0x3800u;
constexpr unsigned NTAB = 0x40A1u - DB0;

constexpr size_t WS_FRAG = 0;
constexpr size_t WS_TAB = 32768;
constexpr size_t WS_CELLA = 131072;
constexpr size_t WS_CELLB = WS_CELLA + (size_t)MAXB * MAXP * 32;
constexpr size_t WS_SORT = WS_CELLB + (size_t)MAXB * MAXP * 32;
constexpr size_t WS_H = WS_SORT + (size_t)MAXP * EPW * 4;
static_assert(WS_TAB + NTAB * 32 <= WS_CELLA, "ws");

constexpr int NQ1 = 13648, NQ2 = NQ1 + 13480, NQ3 = NQ2 + 13648;
constexpr int MAXN = NQ3 + 9384;
constexpr int T_H1 = 80 * 1024;
constexpr int T_STAGE = T_H1 + 55 * 1024;
constexpr int T_CNT = 159 * 1024;
constexpr int T_END = T_CNT + 1024;
static_assert(T_END <= 163840 && T_STAGE + 16384 <= T_CNT, "K1 LDS");
constexpr int T_W1 = 16 * 8192;

constexpr int L_W = 0;
constexpr int L_BUF = NFRAG * 1024;
constexpr int L_LIST = L_BUF + NWIN * 16384;
constexpr int L_SUB = L_LIST + NWIN * 1024;
constexpr int L_CTR = L_SUB + NWIN * 1536;
constexpr int L_END = L_CTR + 256;
static_assert(L_END <= 163840, "K2 LDS");

constexpr int SP0 = 24, SP1 = 24;
constexpr int PFE = 7;
constexpr int LBAR = 3;
constexpr int O_AUX = 2;
constexpr int H_AUX = 16;
#define SB() __builtin_amdgcn_sched_barrier(0)
#define PIN(...) asm volatile("" : __VA_ARGS__)

DI float rb(float x) { return (float)(__bf16)x; }
DI unsigned pk2(float a, float b) { f32x2 v = {a, b}; return __builtin_bit_cast(unsigned, __builtin_convertvector(v, bf16x2)); }
DI float lo16(unsigned u) { return __uint_as_float(u << 16); }
DI float hi16(unsigned u) { return __uint_as_float(u & 0xffff0000u); }
DI u32x2 norm_pos(u32x2 w, unsigned i) {
  const unsigned sh = (i & 1) * 16;
  u32x2 r; r.x = __builtin_amdgcn_alignbit(w.y, w.x, sh); r.y = w.y >> sh; return r;
}
DI unsigned dpp_scan_add(unsigned x) {
  x += __builtin_amdgcn_update_dpp(0u, x, 0x111, 0xf, 0xf, true);
  x += __builtin_amdgcn_update_dpp(0u, x, 0x112, 0xf, 0xf, true);
  x += __builtin_amdgcn_update_dpp(0u, x, 0x114, 0xf, 0xf, true);
  x += __builtin_amdgcn_update_dpp(0u, x, 0x118, 0xf, 0xf, true);
  x += __builtin_amdgcn_update_dpp(0u, x, 0x142, 0xa, 0xf, false);
  x += __builtin_amdgcn_update_dpp(0u, x, 0x143, 0xc, 0xf, false);
  return x;
}
DI int chmap(int b, int i) { return 32 * ((i >> 2) & 1) + 16 * b + (i & 3) + 4 * (i >> 3); }
#define MFMA(a, b, c) __builtin_amdgcn_mfma_f32_32x32x16_bf16((a), (b), (c), 0, 0, 0)
DI bf16x8 ldsf(unsigned a) { return *(const LAS bf16x8*)(size_t)a; }
DI u32x4 lds4(unsigned a) { return *(const LAS u32x4*)(size_t)a; }
DI unsigned lds1(unsigned a) { return *(const LAS unsigned*)(size_t)a; }
DI void sts4(unsigned a, u32x4 v) { *(LAS u32x4*)(size_t)a = v; }
DI void sts1(unsigned a, unsigned v) { *(LAS unsigned*)(size_t)a = v; }
DI unsigned lds_inc(unsigned a) {
  return __hip_atomic_fetch_add((LAS unsigned*)(size_t)a, 1u, __ATOMIC_RELAXED, __HIP_MEMORY_SCOPE_WORKGROUP);
}
DI u32x4 pack8u(const f32x16& x, int s) {
  u32x4 p;
  p.x = pk2(x[8 * s], x[8 * s + 1]); p.y = pk2(x[8 * s + 2], x[8 * s + 3]);
  p.z = pk2(x[8 * s + 4], x[8 * s + 5]); p.w = pk2(x[8 * s + 6], x[8 * s + 7]);
  return p;
}
DI void dma_lin(unsigned m0v, unsigned voff, unsigned soff, __amdgpu_buffer_rsrc_t rsrc) {
  asm volatile("s_mov_b32 m0, %0\n\ts_nop 0\n\tbuffer_load_dwordx4 %1, %2, %3 offen lds"
               :: "s"(m0v), "v"(voff), "s"(rsrc), "s"(soff) : "m0", "memory");
}
DI void dma_lin4(unsigned m0v, unsigned voff, unsigned soff, __amdgpu_buffer_rsrc_t rsrc) {
  asm volatile("s_mov_b32 m0, %0\n\ts_nop 0\n\tbuffer_load_dwordx4 %1, %2, %3 offen lds\n\t"
               "buffer_load_dwordx4 %1, %2, %3 offen offset:1024 lds\n\tbuffer_load_dwordx4 %1, %2, %3 offen offset:2048 lds\n\t"
               "buffer_load_dwordx4 %1, %2, %3 offen offset:3072 lds"
               :: "s"(m0v), "v"(voff), "s"(rsrc), "s"(soff) : "m0", "memory");
}
DI void wg_barrier() { asm volatile("s_waitcnt lgkmcnt(0)\n\ts_barrier" ::: "memory"); }
DI void wave_sync() {
  __builtin_amdgcn_fence(__ATOMIC_RELEASE, "wavefront");
  __builtin_amdgcn_wave_barrier();
  __builtin_amdgcn_fence(__ATOMIC_ACQUIRE, "wavefront");
}
#define CE(a, b) do { const unsigned lo_ = min(a, b), hi_ = max(a, b); a = lo_; b = hi_; } while (0)
DI void sort8(unsigned* v) {
  CE(v[0], v[1]); CE(v[2], v[3]); CE(v[4], v[5]); CE(v[6], v[7]);
  CE(v[0], v[2]); CE(v[1], v[3]); CE(v[4], v[6]); CE(v[5], v[7]);
  CE(v[1], v[2]); CE(v[5], v[6]);
  CE(v[0], v[4]); CE(v[1], v[5]); CE(v[2], v[6]); CE(v[3], v[7]);
  CE(v[2], v[4]); CE(v[3], v[5]);
  CE(v[1], v[2]); CE(v[3], v[4]); CE(v[5], v[6]);
}

DI u32x2 trread(unsigned a) {
  return __builtin_bit_cast(u32x2, __builtin_amdgcn_ds_read_tr16_b64_v4i16((LAS s16x4*)(size_t)a));
}
DI unsigned swz(unsigned row) { return (row & 2u) | ((row >> 2) & 1u) | ((row >> 1) & 4u); }

#define DMA4_(POL)                                                                                           \
  asm volatile(                                                                                              \
      "s_cmp_eq_u32 %8, 0\n\ts_cbranch_scc1 .Lskip%=\n\t"                                                    \
      "s_add_u32 m0, %0, %7\n\ts_waitcnt lgkmcnt(0)\n\t"                                                     \
      "buffer_load_dwordx4 %1, %5, 0 offen offset:%6 " POL "lds\n\t"                                         \
      "buffer_load_dwordx4 %2, %5, 0 offen offset:%9 " POL "lds\n\t"                                         \
      "buffer_load_dwordx4 %3, %5, 0 offen offset:%10 " POL "lds\n\t"                                        \
      "buffer_load_dwordx4 %4, %5, 0 offen offset:%11 " POL "lds\n"                                          \
      ".Lskip%=:"                                                                                            \
      :: "s"(sbuf), "v"(vo.x), "v"(vo.y), "v"(vo.z), "v"(vo.w), "s"(rsrc), "i"(PART * 128), "i"(PART * 3968), "s"(cond), \
         "i"(PART * 128 + 1024), "i"(PART * 128 + 2048), "i"(PART * 128 + 3072)                              \
      : "m0", "scc", "memory")
DI __amdgpu_buffer_rsrc_t biased_rsrc(const char* p, int bytes) {
  return __builtin_amdgcn_make_buffer_rsrc((void*)(p - 3072), (short)0, bytes + 3072, 0x00020000);
}
DI unsigned lane_chunk(int l) { return (unsigned)(((l & 7) ^ (((l >> 4) & 1) << 1) ^ (l >> 5)) * 16); }
DI u32x4 row_offsets(unsigned a, unsigned b, unsigned c, unsigned d, unsigned cb) {
  return (u32x4){a + cb, b + (cb ^ 0x440u), c + (cb ^ 0x800u), d + (cb ^ 0xC40u)};
}
template <int PART, int NT>
DI void dma_part(unsigned cond, unsigned sbuf, u32x4 vo, __amdgpu_buffer_rsrc_t rsrc) {
  if (NT) DMA4_("nt "); else DMA4_("");
}

__global__ __launch_bounds__(K1T) void bin_edges(const char* __restrict__ sv, const char* __restrict__ pos,
                                                 const int* __restrict__ src, const int* __restrict__ dst,
                                                 const uint16_t* __restrict__ freqs, const char* __restrict__ w1,
                                                 const uint16_t* __restrict__ w2, const uint16_t* __restrict__ wr,
                                                 char* __restrict__ ws, int N, int E, int P, int NB) {
  extern __shared__ __attribute__((aligned(128))) char smem[];
  const unsigned lb = (unsigned)(size_t)(LAS char*)smem;
  const int tid = threadIdx.x, l = tid & 63, w = __builtin_amdgcn_readfirstlane(tid >> 6), p = blockIdx.x;
  const __amdgpu_buffer_rsrc_t rsv = biased_rsrc(sv, N * 512);
  const __amdgpu_buffer_rsrc_t rpos = __builtin_amdgcn_make_buffer_rsrc((void*)pos, (short)0, (N * 6 + 3) & ~3, 0x00020000);
  const int e = l & 31, h = l >> 5;
  unsigned dummy = 0, dummy0 = 0;
  if (w == 15 && p < 32) {
    unsigned long long pc; int o1, o2, o3;
    asm volatile("s_getpc_b64 %0\n\ts_mov_b32 %1, painn_code_begin@rel32@lo+4\n\ts_mov_b32 %2, painn_code_end@rel32@lo+12\n\t"
                 "s_mov_b32 %3, painn_code_end2@rel32@lo+20"
                 : "=s"(pc), "=s"(o1), "=s"(o2), "=s"(o3));
    const __amdgpu_buffer_rsrc_t rc = __builtin_amdgcn_make_buffer_rsrc((void*)(pc + (long long)o1), (short)0, max(max(o2, o3) - o1, 0) & ~3, 0x00020000);
    asm volatile("buffer_load_dword %0, %1, %2, 0 offen" : "+v"(dummy0) : "v"(((p >> 3) * 64 + l) * 128), "s"(rc) : "memory");
  }
  asm volatile("painn_code_begin:" :: "s"(sv), "s"(pos), "s"(src), "s"(dst), "s"(w1), "s"(ws), "s"(N), "s"(E), "s"(P));
  const unsigned cs = (unsigned)(((l & 7) ^ (((l >> 4) & 1) << 1) ^ (l >> 5)) * 16);
  const __amdgpu_buffer_rsrc_t rh = __builtin_amdgcn_make_buffer_rsrc((void*)(ws + WS_H), (short)0, N * 128, 0x00020000);
  u32x4 wf[8], ho[4];
  auto h_issue = [&](unsigned sreg, unsigned hrow) {
    const unsigned r0 = hrow * 512u;
    dma_part<0, 0>(1, sreg, row_offsets(r0, r0 + 4096, r0 + 8192, l < 32 ? r0 + 12288 : 0xE0000000u, cs | 0xC00u), rsv);
  };
  auto h_weights = [&]() {
    const unsigned wt = lb + T_W1 + (32 * h + ((l >> 2) & 3)) * 128 + ((l >> 4) & 1) * 32 + (l & 3) * 8;
#pragma unroll
    for (int i = 0; i < 8; ++i) {
      const u32x2 x = trread(wt + (i & 3) * 1024 + (i >> 2) * 64), y = trread(wt + (i & 3) * 1024 + 512 + (i >> 2) * 64);
      wf[i] = (u32x4){x.x, x.y, y.x, y.y};
    }
  };
  auto h_compute = [&](unsigned sreg) {
    const unsigned ra = sreg + e * 128 + ((4 * h) ^ swz(e)) * 16;
    bf16x8 sf[4];
#pragma unroll
    for (int ks = 0; ks < 4; ++ks) sf[ks] = ldsf(ra ^ (ks * 16));
    f32x16 hh[2] = {};
#pragma unroll
    for (int i = 0; i < 8; ++i) hh[i >> 2] = MFMA(__builtin_bit_cast(bf16x8, wf[i]), sf[i & 3], hh[i >> 2]);
#pragma unroll
    for (int jb = 0; jb < 2; ++jb) {
#pragma unroll
      for (int i = 0; i < 16; ++i) {
        const float x = hh[jb][i];
        hh[jb][i] = x * __builtin_amdgcn_rcpf(1.0f + __builtin_amdgcn_exp2f(-1.4426950408889634f * x));
      }
      sts4(ra ^ (jb * 32), pack8u(hh[jb], 0)); sts4(ra ^ (jb * 32 + 16), pack8u(hh[jb], 1));
    }
    wave_sync();
#pragma unroll
    for (int i = 0; i < 4; ++i) ho[i] = lds4(sreg + i * 1024 + l * 16);
  };
  auto h_store = [&](unsigned hrow) {
#pragma unroll
    for (int i = 0; i < 4; ++i) {
      const unsigned o = (i < 3 || l < 32) ? hrow * 128u + i * 1024 + (cs ^ ((i & 1) * 64)) : OOB;
      __builtin_amdgcn_raw_buffer_store_b128(ho[i], rh, o, 0, H_AUX);
    }
  };
  const unsigned q3 = (unsigned)l / 3u, pfo = q3 * 512u + ((unsigned)l - 3u * q3) * 128u + (128u + 3072u);
  auto prefetch = [&](int row0, int nrows) {
    asm volatile("buffer_load_dword %0, %1, %2, %3 offen" : "+v"(dummy) : "v"(l < 3 * nrows ? pfo : OOB), "s"(rsv), "s"(row0 * 512) : "memory");
  };
  if (__builtin_expect(p >= MAXP, 0)) {
    constexpr int PER = (MAXB / 8 * NWIN + NX / 8 - 1) / (NX / 8);
    static_assert(PER <= 32, "two windows per wave");
    const unsigned sreg = __builtin_amdgcn_readfirstlane(lb + w * 8192);
    int row0[2];
#pragma unroll
    for (int k = 0; k < 2; ++k) {
      const int kk = w + 16 * k, u = ((p - MAXP) >> 3) * PER + kk, ub = u / NWIN, bn = (p & 7) + 8 * ub;
      row0[k] = kk < PER && bn < NB ? bn * NPB + (u - ub * NWIN) * NPW : 0x700000;
    }
    if (w < 8) {
      const __amdgpu_buffer_rsrc_t rw1 = __builtin_amdgcn_make_buffer_rsrc((void*)w1, (short)0, 8192, 0x00020000);
      dma_lin(lb + T_W1 + w * 1024, l * 16, w * 1024, rw1);
    }
    static_assert(NX >= NFRAG && NX * 32 >= (int)NTAB && PER <= 30, "side work");
    const int t = (p - MAXP) * 64 + l;
    const bool dofrag = w == 15 && p - MAXP < NFRAG, dotab = w == 14 && (p - MAXP) * 32 < (int)NTAB;
    unsigned fv[8] = {};
    u32x4 fq = {0, 0, 0, 0};
    if (dofrag) {
      const int f = p - MAXP;
      const bool is2 = f < 24;
      const int blk = is2 ? f >> 2 : f - 24;
      const uint16_t* base = (is2 ? w2 + (16 * (f & 3) + 4 * h) * 192 : wr + 8 * h * 192) + 64 * (blk >> 1) + chmap(blk & 1, e);
      const int hs = is2 ? 8 : 4;
#pragma unroll
      for (int jj = 0; jj < 8; ++jj) fv[jj] = base[(hs * (jj >> 2) + (jj & 3)) * 192];
    }
    if (dotab) fq = *(const u32x4*)(freqs + 8 * (l & 1));
    h_issue(sreg, row0[0] + (l >> 3));
    if (w < 8) asm volatile("s_waitcnt vmcnt(4)" ::: "memory");
    asm volatile("s_barrier" ::: "memory");
    h_issue(sreg + 4096, row0[1] + (l >> 3));
    h_weights();
    asm volatile("s_waitcnt vmcnt(4)" ::: "memory");
    h_compute(sreg);
    h_store(row0[0] + (l >> 3));
    if (w + 16 < PER) {
      asm volatile("s_waitcnt vmcnt(4)" ::: "memory");
      h_compute(sreg + 4096);
      h_store(row0[1] + (l >> 3));
    }
    if (dofrag) {
      const u32x4 o = {fv[0] | (fv[1] << 16), fv[2] | (fv[3] << 16), fv[4] | (fv[5] << 16), fv[6] | (fv[7] << 16)};
      *(u32x4*)(ws + WS_FRAG + (size_t)t * 16) = o;
    }
    if (dotab && t < (int)NTAB * 2) {
      const float d = __uint_as_float((DB0 + (unsigned)(t >> 1)) << 16);
      const float tt = rb(rb(3.14159265358979f * d) * 0.2f);
      const float cs = rb(__builtin_amdgcn_cosf(tt * 0.15915494309189535f));
      const float fc = d < 5.0f ? rb(0.5f * rb(cs + 1.0f)) : 0.0f;
      const float rd = __builtin_amdgcn_rcpf(d);
      u32x4 o;
#pragma unroll
      for (int k = 0; k < 4; ++k) {
        const float a0 = rb(d * lo16(fq[k])), a1 = rb(d * hi16(fq[k]));
        const float x0 = __builtin_fmaf(a0, -1.8562245109e-06f, __builtin_amdgcn_fractf(a0 * 0.15915679931640625f));
        const float x1 = __builtin_fmaf(a1, -1.8562245109e-06f, __builtin_amdgcn_fractf(a1 * 0.15915679931640625f));
        o[k] = pk2(rb(rb(__builtin_amdgcn_sinf(x0)) * rd) * fc, rb(rb(__builtin_amdgcn_sinf(x1)) * rd) * fc);
      }
      *(u32x4*)(ws + WS_TAB + (size_t)t * 16) = o;
    }
    return;
  }
  if (__builtin_expect(w >= ET / 64 || p >= P, 0)) {
    if (w < 15) return;
    asm volatile("buffer_load_dword %0, %1, %2, 0 offen\n\tbuffer_load_dword %0, %1, %2, 0 offen offset:128"
                 : "+v"(dummy) : "v"(((p >> 3) * 54 + min(l, 53)) * 256), "s"(rpos) : "memory");
    if (p < P) {
      asm volatile("s_barrier" ::: "memory");
    auto pieces = [&](unsigned ldsb, unsigned srcb, int n) {
#pragma unroll 2
      for (int f = 0; f + 4 <= n; f += 4) dma_lin4(ldsb + f * 1024, l * 16, srcb + f * 1024, rpos);
#pragma unroll
      for (int f = n & ~3; f < n; ++f) dma_lin(ldsb + f * 1024, l * 16, srcb + f * 1024, rpos);
    };
    __builtin_amdgcn_s_setprio(3);
    pieces(lb, 0, 80);
    pieces(lb + T_H1, NQ1 * 6, SP0);
    asm volatile("s_waitcnt vmcnt(%0)\n\ts_barrier" :: "i"(SP0) : "memory");
    pieces(lb + T_H1 + SP0 * 1024, NQ1 * 6 + SP0 * 1024, 79 - SP0);
    asm volatile("s_barrier" ::: "memory");
    pieces(lb, NQ2 * 6, SP1);
    asm volatile("s_waitcnt vmcnt(%0)\n\ts_barrier" :: "i"(SP1) : "memory");
    pieces(lb + SP1 * 1024, NQ2 * 6 + SP1 * 1024, 80 - SP1);
    asm volatile("s_barrier" ::: "memory");
    pieces(lb + T_H1, NQ3 * 6, SP1);
    asm volatile("s_waitcnt vmcnt(%0)\n\ts_barrier" :: "i"(SP1) : "memory");
    pieces(lb + T_H1 + SP1 * 1024, NQ3 * 6 + SP1 * 1024, 55 - SP1);
#pragma unroll
      for (int j = 0; j < PFE; ++j) {
        const int t = (p >> 3) * PFE + j;
        prefetch(((p & 7) + 8 * (t & 31)) * NPB + (t >> 5) * 21, min(21, NPB - (t >> 5) * 21));
      }
      asm volatile("s_waitcnt vmcnt(%0)\n\ts_barrier" :: "i"(PFE) : "memory");
      asm volatile("s_barrier" ::: "memory");
    }
    asm volatile("s_waitcnt vmcnt(0)" :: "v"(dummy), "v"(dummy0) : "memory");
    return;
  }
  const unsigned e0 = (unsigned)(p * EPW + tid * 4), e1 = (unsigned)(p * EPW + ET * 4 + tid * 2);
  const int nv0 = E - (int)e0, nv1 = E - (int)e1;
  int ix[2 * KE];
  {
    const __amdgpu_buffer_rsrc_t rs = __builtin_amdgcn_make_buffer_rsrc((void*)src, (short)0, E * 4, 0x00020000);
    const __amdgpu_buffer_rsrc_t rd = __builtin_amdgcn_make_buffer_rsrc((void*)dst, (short)0, E * 4, 0x00020000);
    const u32x4 s4 = __builtin_amdgcn_raw_buffer_load_b128(rs, e0 * 4, 0, 0), d4 = __builtin_amdgcn_raw_buffer_load_b128(rd, e0 * 4, 0, 0);
    const u32x2 s2 = __builtin_amdgcn_raw_buffer_load_b64(rs, e1 * 4, 0, 0), d2 = __builtin_amdgcn_raw_buffer_load_b64(rd, e1 * 4, 0, 0);
    asm volatile("s_barrier" ::: "memory");
    if (tid < MAXB) sts1(lb + T_CNT + tid * 4, 0u);
#pragma unroll
    for (int k = 0; k < KE; ++k) { ix[k] = k < 4 ? s4[k & 3] : s2[k & 1]; ix[KE + k] = k < 4 ? d4[k & 3] : d2[k & 1]; }
  }
  u32x2 pp[2 * KE];
  unsigned ad[2 * KE], rec[KE], bin[KE], slot[KE];
  unsigned long long hi[2 * KE], odd[2 * KE];
#pragma unroll
  for (int k = 0; k < 2 * KE; ++k) {
    const bool up = ix[k] >= NQ2;
    const unsigned a6 = ((6u * ix[k]) & ~3u) - (up ? NQ2 * 6 : 0);
    hi[k] = __ballot(up); odd[k] = __ballot(a6 >= (unsigned)(NQ1 * 6));
    ad[k] = lb + a6;
    PIN("+v"(ad[k]), "=v"(pp[k]), "+s"(hi[k]), "+s"(odd[k]));
  }
#pragma unroll
  for (int k = 0; k < KE; ++k) {
    const unsigned d = (unsigned)ix[KE + k], W = d / (unsigned)NPW;
    bin[k] = W / (unsigned)NWIN;
    rec[k] = ((W - bin[k] * NWIN) << 29) | ((d - W * NPW) << 24) | (unsigned)ix[k];
    bin[k] *= 4;
    ix[k] = (ix[k] & 1) * 16; ix[KE + k] = (ix[KE + k] & 1) * 16;
    PIN("+v"(rec[k]), "+v"(bin[k]), "+v"(ix[k]), "+v"(ix[KE + k]));
  }
#define LK1(OP, OFS, d, a, x, y) OP " exec, %" #x ", %" #y "\n\tds_read2_b32 %" #d ", %" #a " " OFS "\n\t"
#define LOOKUP(OP, A, B, OFS)                                                                               \
  _Pragma("unroll") for (int k = 0; k < 2 * KE; k += 6)                                                     \
    asm volatile(LK1(OP, OFS, 0, 6, 12, 18) LK1(OP, OFS, 1, 7, 13, 19) LK1(OP, OFS, 2, 8, 14, 20)           \
                 LK1(OP, OFS, 3, 9, 15, 21) LK1(OP, OFS, 4, 10, 16, 22) LK1(OP, OFS, 5, 11, 17, 23)         \
                 "s_mov_b64 exec, -1"                                                                       \
                 : "+v"(pp[k]), "+v"(pp[k + 1]), "+v"(pp[k + 2]), "+v"(pp[k + 3]), "+v"(pp[k + 4]), "+v"(pp[k + 5]) \
                 : "v"(ad[k]), "v"(ad[k + 1]), "v"(ad[k + 2]), "v"(ad[k + 3]), "v"(ad[k + 4]), "v"(ad[k + 5]),      \
                   "s"(A[k]), "s"(A[k + 1]), "s"(A[k + 2]), "s"(A[k + 3]), "s"(A[k + 4]), "s"(A[k + 5]),            \
                   "s"(B[k]), "s"(B[k + 1]), "s"(B[k + 2]), "s"(B[k + 3]), "s"(B[k + 4]), "s"(B[k + 5])             \
                 : "scc", "memory")
  static_assert(T_H1 - NQ1 * 6 == 32, "lookup offsets");
  SB();
  wg_barrier();
  SB(); LOOKUP("s_nor_b64", hi, odd, "offset1:1"); SB();
  asm volatile("s_waitcnt lgkmcnt(0)\n\ts_barrier\n\ts_barrier" ::: "memory");
  SB(); LOOKUP("s_andn2_b64", odd, hi, "offset0:8 offset1:9"); SB();
  asm volatile("s_waitcnt lgkmcnt(0)\n\ts_barrier\n\ts_barrier" ::: "memory");
  SB(); LOOKUP("s_andn2_b64", hi, odd, "offset1:1"); SB();
  {
    const u32x4 sent = {SENT, SENT, SENT, SENT};
    sts4(lb + T_STAGE + tid * 16, sent);
    if (tid < 1024 - ET) sts4(lb + T_STAGE + (ET + tid) * 16, sent);
  }
  wg_barrier();
  SB(); LOOKUP("s_and_b64", hi, odd, "offset0:8 offset1:9"); SB();
  asm volatile("s_waitcnt lgkmcnt(0)" : "+v"(pp[0]), "+v"(pp[1]), "+v"(pp[2]), "+v"(pp[3]), "+v"(pp[4]), "+v"(pp[5]),
               "+v"(pp[6]), "+v"(pp[7]), "+v"(pp[8]), "+v"(pp[9]), "+v"(pp[10]), "+v"(pp[11]) :: "memory");
  bool val[KE];
#pragma unroll
  for (int k = 0; k < KE; ++k) {
    const bf16x2 axy = __builtin_bit_cast(bf16x2, __builtin_amdgcn_alignbit(pp[k].y, pp[k].x, ix[k]));
    const bf16x2 az = __builtin_bit_cast(bf16x2, __builtin_amdgcn_ubfe(pp[k].y, ix[k], 16));
    const bf16x2 bxy = __builtin_bit_cast(bf16x2, __builtin_amdgcn_alignbit(pp[KE + k].y, pp[KE + k].x, ix[KE + k]));
    const bf16x2 bz = __builtin_bit_cast(bf16x2, __builtin_amdgcn_ubfe(pp[KE + k].y, ix[KE + k], 16));
    float S = __builtin_amdgcn_fdot2_f32_bf16(axy, axy, 0.0f, false);
    S = __builtin_amdgcn_fdot2_f32_bf16(az, az, S, false);
    S = __builtin_amdgcn_fdot2_f32_bf16(bxy, bxy, S, false);
    S = __builtin_amdgcn_fdot2_f32_bf16(bz, bz, S, false);
    float T = __builtin_amdgcn_fdot2_f32_bf16(axy, bxy, 0.0f, false);
    T = __builtin_amdgcn_fdot2_f32_bf16(az, bz, T, false);
    const float t = __builtin_fmaf(S, 0.49999f, -T);
    val[k] = !((t >= 12.12f) & (t < __builtin_inff())) & (k < 4 ? k < nv0 : k - 4 < nv1);
  }
#pragma unroll
  for (int k = 0; k < KE; ++k) slot[k] = lds_inc((val[k] ? bin[k] : 0x40000u) + (lb + T_CNT));
#pragma unroll
  for (int k = 0; k < KE; ++k) sts1(((val[k] & (slot[k] < 16)) ? bin[k] * 16 + slot[k] * 4 : 0x40000u) + (lb + T_STAGE), rec[k]);
  wg_barrier();
  const unsigned myc = tid < MAXB ? lds1(lb + T_CNT + tid * 4) : 0u;
  const u32x4 call = lds4(lb + T_CNT + l * 16);
  const unsigned cmax = max(max(call.x, call.y), max(call.z, call.w));
  const unsigned long long big8 = __ballot(cmax > 8u);
  const bool big15 = __any(cmax > 15u);
  if (big15) {
    const unsigned t_off = lb, t_ws = lb + 1024, t_tmp = lb + 2048, t_bin = t_tmp + EPW * 4;
    const unsigned inc = dpp_scan_add(myc);
    if (tid < MAXB && l == 63) sts1(t_ws + w * 4, inc);
    wg_barrier();
    if (tid < MAXB) {
      unsigned myo = inc - myc;
      for (int i = 0; i < w; ++i) myo += lds1(t_ws + i * 4);
      sts1(t_off + tid * 4, myo);
    }
    wg_barrier();
#pragma unroll
    for (int k = 0; k < KE; ++k)
      if (val[k]) {
        const unsigned o = (lds1(t_off + bin[k]) + slot[k]) * 4;
        sts1(t_tmp + o, rec[k]); sts1(t_bin + o, bin[k]);
      }
    wg_barrier();
    const u32x4 ws4 = lds4(t_ws);
#pragma unroll 1
    for (unsigned i = tid; i < ws4.x + ws4.y + ws4.z + ws4.w; i += ET) {
      const unsigned rk = lds1(t_tmp + i * 4), bk = lds1(t_bin + i * 4);
      const unsigned o = lds1(t_off + bk), c = lds1(lb + T_CNT + bk);
      unsigned rank = 0;
#pragma unroll 1
      for (unsigned j = o; j < o + c; ++j) {
        const unsigned x = lds1(t_tmp + j * 4);
        rank += (x < rk || (x == rk && j < i)) ? 1u : 0u;
      }
      ((unsigned*)(ws + WS_SORT))[(size_t)p * EPW + min(o + rank, (unsigned)EPW - 1)] = rk;
    }
  }
  char* cbase = ws + WS_CELLA + ((p & 7) * PSL + (p >> 3)) * 32;
  if (tid < MAXB) {
    if (tid < NB && myc <= 8) {
      unsigned v[8];
      const u32x4 a = lds4(lb + T_STAGE + tid * 64), b = lds4(lb + T_STAGE + tid * 64 + 16);
      v[0] = a.x; v[1] = a.y; v[2] = a.z; v[3] = a.w; v[4] = b.x; v[5] = b.y; v[6] = b.z; v[7] = b.w;
      sort8(v);
      char* ca = cbase + (size_t)tid * (MAXP * 32);
      const u32x4 x = {v[0], v[1], v[2], v[3]}, y = {v[4], v[5], v[6], v[7]};
      *(u32x4*)ca = x; *(u32x4*)(ca + 16) = y;
    }
  } else {
    const unsigned j = l & 15;
    static_assert(ET == 768, "waves 4..11 take 32 bins each");
#pragma unroll 1
    for (unsigned m = (unsigned)(big8 >> (8 * (w - 4))) & 0xffu; m; m &= m - 1) {
      const unsigned bn = ((w - 4) * 8 + __builtin_ctz(m)) * 4 + (l >> 4);
      const unsigned c = lds1(lb + T_CNT + bn * 4);
      const unsigned x = lds1(lb + T_STAGE + bn * 64 + j * 4);
      const unsigned key = (x & 0xff000000u) | ((x << 4) & 0xfffff0u) | j;
      unsigned rank = 0;
#define ROR(r) rank += __builtin_amdgcn_update_dpp(0u, key, 0x120 + r, 0xf, 0xf, true) < key ? 1u : 0u;
      ROR(1) ROR(2) ROR(3) ROR(4) ROR(5) ROR(6) ROR(7) ROR(8) ROR(9) ROR(10) ROR(11) ROR(12) ROR(13) ROR(14) ROR(15)
      if (c > 8) {
        unsigned* ca = (unsigned*)(cbase + (size_t)bn * (MAXP * 32));
        if (rank == 15) ca[7] = 0xE0000000u | ((big15 ? lds1(lb + bn * 4) : 0u) << 13) | c;
        else ca[rank < 7 ? rank : rank - 7 + (WS_CELLB - WS_CELLA) / 4] = x;
      }
    }
  }
}


#define NPRE 1
#define PRE_WAIT "s_waitcnt vmcnt(4) lgkmcnt(0)\n"
#define TILE_LOOP_ASM \
  "s_mov_b64 vcc, -1\n" \
  "s_cmp_lt_u32 %[nt], 2\n" \
  "s_cbranch_scc1 .Llast%=\n" \
  ".Lloop%=:\n" \
  "s_setprio 1\n" \
  "ds_read_b128 v[80:83], v5 offset:0\n" \
  "ds_read_b128 v[84:87], v5 offset:32\n" \
  "v_xor_b32 v9, 16, v1\n" \
  "v_xor_b32 v10, 32, v1\n" \
  "v_xor_b32 v11, 48, v1\n" \
  "s_waitcnt vmcnt(12)\n" \
  "ds_read_b128 v[16:19], v1 offset:0\n" \
  "ds_read_b128 v[32:35], v2 offset:16384\n" \
  "ds_read_b128 v[20:23], v9 offset:0\n" \
  "ds_read_b128 v[36:39], v2 offset:17408\n" \
  "ds_read_b128 v[24:27], v10 offset:0\n" \
  "ds_read_b128 v[40:43], v2 offset:18432\n" \
  "ds_read_b128 v[28:31], v11 offset:0\n" \
  "ds_read_b128 v[44:47], v2 offset:19456\n" \
  "ds_read_b128 v[120:123], v5 offset:128\n" \
  "ds_read_b128 v[124:127], v5 offset:160\n" \
  "ds_read2_b32 v[104:105], v6 offset0:32 offset1:40\n" \
  "ds_read2_b32 v[106:107], v6 offset0:48 offset1:56\n" \
  "s_waitcnt lgkmcnt(13)\n" \
  "v_xor_b32 v96, v80, v4\n" \
  "v_pk_sub_u16 v96, 1, v96 op_sel_hi:[0,1] clamp\n" \
  "v_xor_b32 v97, v81, v4\n" \
  "v_pk_sub_u16 v97, 1, v97 op_sel_hi:[0,1] clamp\n" \
  "v_xor_b32 v98, v82, v4\n" \
  "v_pk_sub_u16 v98, 1, v98 op_sel_hi:[0,1] clamp\n" \
  "v_xor_b32 v99, v83, v4\n" \
  "v_pk_sub_u16 v99, 1, v99 op_sel_hi:[0,1] clamp\n" \
  "s_waitcnt lgkmcnt(10)\n" \
  "v_mfma_f32_32x32x16_bf16 v[48:63], v[16:19], v[32:35], 0\n" \
  "s_cbranch_vccz .Lfn2%=\n" \
  "s_add_u32 m0, %[buf], 7936\n" \
  "s_nop 0\n" \
  "buffer_load_dwordx4 v112, %[rg], 0 offen offset:256 lds\n" \
  "buffer_load_dwordx4 v113, %[rg], 0 offen offset:1280 lds\n" \
  "buffer_load_dwordx4 v114, %[rg], 0 offen offset:2304 lds\n" \
  "buffer_load_dwordx4 v115, %[rg], 0 offen offset:3328 lds\n" \
  ".Lfn2%=:\n" \
  "ds_read_b128 v[32:35], v2 offset:28672\n" \
  "v_xor_b32 v100, v84, v4\n" \
  "v_pk_sub_u16 v100, 1, v100 op_sel_hi:[0,1] clamp\n" \
  "v_xor_b32 v101, v85, v4\n" \
  "v_pk_sub_u16 v101, 1, v101 op_sel_hi:[0,1] clamp\n" \
  "v_xor_b32 v102, v86, v4\n" \
  "v_pk_sub_u16 v102, 1, v102 op_sel_hi:[0,1] clamp\n" \
  "s_waitcnt lgkmcnt(9)\n" \
  "v_mfma_f32_32x32x16_bf16 v[48:63], v[20:23], v[36:39], v[48:63]\n" \
  "s_cbranch_vccz .Lfn3%=\n" \
  "s_add_u32 m0, %[buf], 11904\n" \
  "s_nop 0\n" \
  "buffer_load_dwordx4 v112, %[rg], 0 offen offset:384 lds\n" \
  "buffer_load_dwordx4 v113, %[rg], 0 offen offset:1408 lds\n" \
  "buffer_load_dwordx4 v114, %[rg], 0 offen offset:2432 lds\n" \
  "buffer_load_dwordx4 v115, %[rg], 0 offen offset:3456 lds\n" \
  ".Lfn3%=:\n" \
  "ds_read_b128 v[36:39], v2 offset:20480\n" \
  "v_xor_b32 v103, v87, v4\n" \
  "v_pk_sub_u16 v103, 1, v103 op_sel_hi:[0,1] clamp\n" \
  "s_waitcnt lgkmcnt(5)\n" \
  "v_pk_mul_lo_u16 v120, v96, v120\n" \
  "v_pk_mul_lo_u16 v121, v97, v121\n" \
  "v_pk_mul_lo_u16 v122, v98, v122\n" \
  "v_pk_mul_lo_u16 v123, v99, v123\n" \
  "v_mfma_f32_32x32x16_bf16 v[48:63], v[24:27], v[40:43], v[48:63]\n" \
  "ds_read_b128 v[40:43], v2 offset:21504\n" \
  "s_waitcnt lgkmcnt(5)\n" \
  "v_pk_mul_lo_u16 v124, v100, v124\n" \
  "v_pk_mul_lo_u16 v125, v101, v125\n" \
  "v_pk_mul_lo_u16 v126, v102, v126\n" \
  "v_pk_mul_lo_u16 v127, v103, v127\n" \
  "v_xor_b32 v9, 0x440, v0\n" \
  "v_xor_b32 v10, 0x800, v0\n" \
  "v_mfma_f32_32x32x16_bf16 v[48:63], v[28:31], v[44:47], v[48:63]\n" \
  "ds_read_b128 v[44:47], v2 offset:22528\n" \
  "v_xor_b32 v11, 0xc40, v0\n" \
  "s_waitcnt lgkmcnt(5)\n" \
  "v_and_b32 v108, 0xffffff, v104\n" \
  "v_lshl_add_u32 v108, v108, 7, v0\n" \
  "v_and_b32 v109, 0xffffff, v105\n" \
  "v_lshl_add_u32 v109, v109, 7, v9\n" \
  "s_waitcnt lgkmcnt(4)\n" \
  "v_and_b32 v110, 0xffffff, v106\n" \
  "s_waitcnt lgkmcnt(3)\n" \
  "v_mfma_f32_32x32x16_bf16 v[64:79], v[12:15], v[32:35], 0\n" \
  "ds_read_b128 v[32:35], v2 offset:23552\n" \
  "v_lshl_add_u32 v110, v110, 7, v10\n" \
  "v_and_b32 v111, 0xffffff, v107\n" \
  "v_lshl_add_u32 v111, v111, 7, v11\n" \
  "s_mov_b64 vcc, 0\n" \
  "s_waitcnt lgkmcnt(3)\n" \
  "v_mfma_f32_32x32x16_bf16 v[80:95], v[16:19], v[36:39], 0\n" \
  "ds_read_b128 v[36:39], v2 offset:29696\n" \
  "s_add_u32 m0, %[buf], 0\n" \
  "s_nop 0\n" \
  "buffer_load_dwordx4 v108, %[rh], 0 offen offset:0 lds\n" \
  "buffer_load_dwordx4 v109, %[rh], 0 offen offset:1024 lds\n" \
  "buffer_load_dwordx4 v110, %[rh], 0 offen offset:2048 lds\n" \
  "s_waitcnt lgkmcnt(3)\n" \
  "v_mfma_f32_32x32x16_bf16 v[80:95], v[20:23], v[40:43], v[80:95]\n" \
  "ds_read_b128 v[40:43], v2 offset:0\n" \
  "buffer_load_dwordx4 v111, %[rh], 0 offen offset:3072 lds\n" \
  "v_mul_f32 v48, v48, v64\n" \
  "v_mul_f32 v49, v49, v65\n" \
  "v_mul_f32 v50, v50, v66\n" \
  "v_mul_f32 v51, v51, v67\n" \
  "s_waitcnt lgkmcnt(3)\n" \
  "v_mfma_f32_32x32x16_bf16 v[80:95], v[24:27], v[44:47], v[80:95]\n" \
  "ds_read_b128 v[44:47], v2 offset:1024\n" \
  "v_mul_f32 v52, v52, v68\n" \
  "v_mul_f32 v53, v53, v69\n" \
  "v_mul_f32 v54, v54, v70\n" \
  "v_mul_f32 v55, v55, v71\n" \
  "v_mul_f32 v56, v56, v72\n" \
  "v_mul_f32 v57, v57, v73\n" \
  "s_waitcnt lgkmcnt(3)\n" \
  "v_mfma_f32_32x32x16_bf16 v[80:95], v[28:31], v[32:35], v[80:95]\n" \
  "ds_read_b128 v[32:35], v2 offset:2048\n" \
  "v_mul_f32 v58, v58, v74\n" \
  "v_mul_f32 v59, v59, v75\n" \
  "v_mul_f32 v60, v60, v76\n" \
  "v_mul_f32 v61, v61, v77\n" \
  "v_mul_f32 v62, v62, v78\n" \
  "v_mul_f32 v63, v63, v79\n" \
  "s_waitcnt lgkmcnt(3)\n" \
  "v_mfma_f32_32x32x16_bf16 v[64:79], v[12:15], v[36:39], 0\n" \
  "ds_read_b128 v[36:39], v2 offset:3072\n" \
  "v_cvt_pk_bf16_f32 v104, v48, v49\n" \
  "v_cvt_pk_bf16_f32 v105, v50, v51\n" \
  "v_cvt_pk_bf16_f32 v106, v52, v53\n" \
  "v_cvt_pk_bf16_f32 v107, v54, v55\n" \
  "v_cvt_pk_bf16_f32 v108, v56, v57\n" \
  "v_cvt_pk_bf16_f32 v109, v58, v59\n" \
  "v_cvt_pk_bf16_f32 v110, v60, v61\n" \
  "v_cvt_pk_bf16_f32 v111, v62, v63\n" \
  "s_waitcnt lgkmcnt(3)\n" \
  "v_mfma_f32_32x32x16_bf16 v[48:63], v[16:19], v[40:43], 0\n" \
  "ds_read_b128 v[40:43], v2 offset:24576\n" \
  "s_waitcnt lgkmcnt(3)\n" \
  "v_mfma_f32_32x32x16_bf16 v[48:63], v[20:23], v[44:47], v[48:63]\n" \
  "ds_read_b128 v[44:47], v2 offset:4096\n" \
  "v_mul_f32 v80, v80, v64\n" \
  "v_mul_f32 v81, v81, v65\n" \
  "v_mul_f32 v82, v82, v66\n" \
  "v_mul_f32 v83, v83, v67\n" \
  "v_mul_f32 v84, v84, v68\n" \
  "v_mul_f32 v85, v85, v69\n" \
  "s_waitcnt lgkmcnt(3)\n" \
  "v_mfma_f32_32x32x16_bf16 v[48:63], v[24:27], v[32:35], v[48:63]\n" \
  "ds_read_b128 v[32:35], v2 offset:5120\n" \
  "v_mul_f32 v86, v86, v70\n" \
  "v_mul_f32 v87, v87, v71\n" \
  "v_mul_f32 v88, v88, v72\n" \
  "v_mul_f32 v89, v89, v73\n" \
  "v_mul_f32 v90, v90, v74\n" \
  "v_mul_f32 v91, v91, v75\n" \
  "s_waitcnt lgkmcnt(3)\n" \
  "v_mfma_f32_32x32x16_bf16 v[48:63], v[28:31], v[36:39], v[48:63]\n" \
  "ds_read_b128 v[36:39], v2 offset:6144\n" \
  "v_mul_f32 v92, v92, v76\n" \
  "v_mul_f32 v93, v93, v77\n" \
  "v_mul_f32 v94, v94, v78\n" \
  "v_mul_f32 v95, v95, v79\n" \
  "s_waitcnt lgkmcnt(3)\n" \
  "v_mfma_f32_32x32x16_bf16 v[64:79], v[12:15], v[40:43], 0\n" \
  "ds_read_b128 v[40:43], v2 offset:7168\n" \
  "v_cvt_pk_bf16_f32 v112, v80, v81\n" \
  "v_cvt_pk_bf16_f32 v113, v82, v83\n" \
  "v_cvt_pk_bf16_f32 v114, v84, v85\n" \
  "v_cvt_pk_bf16_f32 v115, v86, v87\n" \
  "v_cvt_pk_bf16_f32 v116, v88, v89\n" \
  "v_cvt_pk_bf16_f32 v117, v90, v91\n" \
  "v_cvt_pk_bf16_f32 v118, v92, v93\n" \
  "v_cvt_pk_bf16_f32 v119, v94, v95\n" \
  "ds_read_b128 v[80:83], v5 offset:192\n" \
  "ds_read_b128 v[84:87], v5 offset:224\n" \
  "ds_read_b128 v[88:91], v5 offset:256\n" \
  "ds_read_b128 v[92:95], v5 offset:288\n" \
  "v_mfma_f32_32x32x16_bf16 v[160:175], v[104:107], v[120:123], v[160:175]\n" \
  "v_mfma_f32_32x32x16_bf16 v[176:191], v[112:115], v[120:123], v[176:191]\n" \
  "s_waitcnt lgkmcnt(3)\n" \
  "v_pk_mul_lo_u16 v120, v96, v80\n" \
  "v_pk_mul_lo_u16 v121, v97, v81\n" \
  "v_pk_mul_lo_u16 v122, v98, v82\n" \
  "v_pk_mul_lo_u16 v123, v99, v83\n" \
  "v_mfma_f32_32x32x16_bf16 v[160:175], v[108:111], v[124:127], v[160:175]\n" \
  "v_mul_f32 v48, v48, v64\n" \
  "v_mul_f32 v49, v49, v65\n" \
  "v_mul_f32 v50, v50, v66\n" \
  "v_mul_f32 v51, v51, v67\n" \
  "v_mul_f32 v52, v52, v68\n" \
  "v_mul_f32 v53, v53, v69\n" \
  "v_mfma_f32_32x32x16_bf16 v[176:191], v[116:119], v[124:127], v[176:191]\n" \
  "s_waitcnt lgkmcnt(2)\n" \
  "v_pk_mul_lo_u16 v124, v100, v84\n" \
  "v_pk_mul_lo_u16 v125, v101, v85\n" \
  "v_pk_mul_lo_u16 v126, v102, v86\n" \
  "v_pk_mul_lo_u16 v127, v103, v87\n" \
  "v_mul_f32 v54, v54, v70\n" \
  "v_mul_f32 v55, v55, v71\n" \
  "v_mfma_f32_32x32x16_bf16 v[192:207], v[104:107], v[120:123], v[192:207]\n" \
  "v_mul_f32 v56, v56, v72\n" \
  "v_mul_f32 v57, v57, v73\n" \
  "v_mul_f32 v58, v58, v74\n" \
  "v_mul_f32 v59, v59, v75\n" \
  "v_mul_f32 v60, v60, v76\n" \
  "v_mul_f32 v61, v61, v77\n" \
  "v_mfma_f32_32x32x16_bf16 v[208:223], v[112:115], v[120:123], v[208:223]\n" \
  "s_waitcnt lgkmcnt(1)\n" \
  "v_pk_mul_lo_u16 v120, v96, v88\n" \
  "v_pk_mul_lo_u16 v121, v97, v89\n" \
  "v_pk_mul_lo_u16 v122, v98, v90\n" \
  "v_pk_mul_lo_u16 v123, v99, v91\n" \
  "v_mul_f32 v62, v62, v78\n" \
  "v_mul_f32 v63, v63, v79\n" \
  "v_mfma_f32_32x32x16_bf16 v[192:207], v[108:111], v[124:127], v[192:207]\n" \
  "v_cvt_pk_bf16_f32 v48, v48, v49\n" \
  "v_cvt_pk_bf16_f32 v49, v50, v51\n" \
  "v_cvt_pk_bf16_f32 v50, v52, v53\n" \
  "v_cvt_pk_bf16_f32 v51, v54, v55\n" \
  "v_cvt_pk_bf16_f32 v52, v56, v57\n" \
  "v_cvt_pk_bf16_f32 v53, v58, v59\n" \
  "v_mfma_f32_32x32x16_bf16 v[208:223], v[116:119], v[124:127], v[208:223]\n" \
  "s_waitcnt lgkmcnt(0)\n" \
  "v_pk_mul_lo_u16 v124, v100, v92\n" \
  "v_pk_mul_lo_u16 v125, v101, v93\n" \
  "v_pk_mul_lo_u16 v126, v102, v94\n" \
  "v_pk_mul_lo_u16 v127, v103, v95\n" \
  "v_cvt_pk_bf16_f32 v54, v60, v61\n" \
  "v_cvt_pk_bf16_f32 v55, v62, v63\n" \
  "v_mfma_f32_32x32x16_bf16 v[224:239], v[104:107], v[120:123], v[224:239]\n" \
  "v_mfma_f32_32x32x16_bf16 v[240:255], v[112:115], v[120:123], v[240:255]\n" \
  "v_mfma_f32_32x32x16_bf16 v[224:239], v[108:111], v[124:127], v[224:239]\n" \
  "v_mfma_f32_32x32x16_bf16 v[240:255], v[116:119], v[124:127], v[240:255]\n" \
  "ds_read_b128 v[120:123], v5 offset:64\n" \
  "ds_read_b128 v[124:127], v5 offset:96\n" \
  "v_mfma_f32_32x32x16_bf16 v[80:95], v[16:19], v[44:47], 0\n" \
  "ds_read_b128 v[44:47], v2 offset:25600\n" \
  "v_mfma_f32_32x32x16_bf16 v[80:95], v[20:23], v[32:35], v[80:95]\n" \
  "ds_read_b128 v[32:35], v2 offset:8192\n" \
  "v_mfma_f32_32x32x16_bf16 v[80:95], v[24:27], v[36:39], v[80:95]\n" \
  "ds_read_b128 v[36:39], v2 offset:9216\n" \
  "s_waitcnt lgkmcnt(4)\n" \
  "v_pk_mul_lo_u16 v96, v96, v120\n" \
  "v_pk_mul_lo_u16 v97, v97, v121\n" \
  "v_pk_mul_lo_u16 v98, v98, v122\n" \
  "v_pk_mul_lo_u16 v99, v99, v123\n" \
  "s_waitcnt lgkmcnt(3)\n" \
  "v_pk_mul_lo_u16 v100, v100, v124\n" \
  "v_pk_mul_lo_u16 v101, v101, v125\n" \
  "v_mfma_f32_32x32x16_bf16 v[80:95], v[28:31], v[40:43], v[80:95]\n" \
  "ds_read_b128 v[40:43], v2 offset:10240\n" \
  "v_pk_mul_lo_u16 v102, v102, v126\n" \
  "v_pk_mul_lo_u16 v103, v103, v127\n" \
  "s_waitcnt lgkmcnt(3)\n" \
  "v_mfma_f32_32x32x16_bf16 v[64:79], v[12:15], v[44:47], 0\n" \
  "ds_read_b128 v[44:47], v2 offset:11264\n" \
  "ds_read2_b32 v[120:121], v6 offset0:32 offset1:40\n" \
  "ds_read2_b32 v[122:123], v6 offset0:48 offset1:56\n" \
  "ds_read_u16 v124, v7 offset:384\n" \
  "v_mfma_f32_32x32x16_bf16 v[128:143], v[48:51], v[96:99], v[128:143]\n" \
  "v_mfma_f32_32x32x16_bf16 v[128:143], v[52:55], v[100:103], v[128:143]\n" \
  "s_waitcnt lgkmcnt(6)\n" \
  "v_mfma_f32_32x32x16_bf16 v[48:63], v[16:19], v[32:35], 0\n" \
  "ds_read_b128 v[32:35], v2 offset:26624\n" \
  "s_nop 2\n" \
  "v_mul_f32 v80, v80, v64\n" \
  "v_mul_f32 v81, v81, v65\n" \
  "v_mul_f32 v82, v82, v66\n" \
  "v_mul_f32 v83, v83, v67\n" \
  "v_mul_f32 v84, v84, v68\n" \
  "v_mul_f32 v85, v85, v69\n" \
  "s_waitcnt lgkmcnt(6)\n" \
  "v_mfma_f32_32x32x16_bf16 v[48:63], v[20:23], v[36:39], v[48:63]\n" \
  "ds_read_b128 v[36:39], v2 offset:12288\n" \
  "v_mul_f32 v86, v86, v70\n" \
  "v_mul_f32 v87, v87, v71\n" \
  "v_mul_f32 v88, v88, v72\n" \
  "v_mul_f32 v89, v89, v73\n" \
  "v_mul_f32 v90, v90, v74\n" \
  "v_mul_f32 v91, v91, v75\n" \
  "s_waitcnt lgkmcnt(6)\n" \
  "v_mfma_f32_32x32x16_bf16 v[48:63], v[24:27], v[40:43], v[48:63]\n" \
  "ds_read_b128 v[40:43], v2 offset:13312\n" \
  "v_mul_f32 v92, v92, v76\n" \
  "v_mul_f32 v93, v93, v77\n" \
  "v_mul_f32 v94, v94, v78\n" \
  "v_mul_f32 v95, v95, v79\n" \
  "s_waitcnt lgkmcnt(6)\n" \
  "v_mfma_f32_32x32x16_bf16 v[48:63], v[28:31], v[44:47], v[48:63]\n" \
  "ds_read_b128 v[44:47], v2 offset:14336\n" \
  "s_waitcnt lgkmcnt(3)\n" \
  "v_mfma_f32_32x32x16_bf16 v[64:79], v[12:15], v[32:35], 0\n" \
  "ds_read_b128 v[32:35], v2 offset:15360\n" \
  "v_cvt_pk_bf16_f32 v80, v80, v81\n" \
  "v_cvt_pk_bf16_f32 v81, v82, v83\n" \
  "v_cvt_pk_bf16_f32 v82, v84, v85\n" \
  "v_cvt_pk_bf16_f32 v83, v86, v87\n" \
  "v_cvt_pk_bf16_f32 v84, v88, v89\n" \
  "v_cvt_pk_bf16_f32 v85, v90, v91\n" \
  "v_cvt_pk_bf16_f32 v86, v92, v93\n" \
  "v_cvt_pk_bf16_f32 v87, v94, v95\n" \
  "v_mfma_f32_32x32x16_bf16 v[144:159], v[80:83], v[96:99], v[144:159]\n" \
  "v_lshl_add_u32 v120, v120, 9, v0\n" \
  "v_lshl_add_u32 v121, v121, 9, v9\n" \
  "v_lshl_add_u32 v122, v122, 9, v10\n" \
  "v_lshl_add_u32 v123, v123, 9, v11\n" \
  "v_lshl_add_u32 v124, v124, 5, v8\n" \
  "v_xor_b32 v9, 32, v3\n" \
  "v_mfma_f32_32x32x16_bf16 v[144:159], v[84:87], v[100:103], v[144:159]\n" \
  "v_xor_b32 v10, 64, v3\n" \
  "v_xor_b32 v11, 96, v3\n" \
  "s_waitcnt lgkmcnt(3)\n" \
  "v_mfma_f32_32x32x16_bf16 v[80:95], v[16:19], v[36:39], 0\n" \
  "ds_read_b128 v[36:39], v2 offset:27648\n" \
  "v_mul_f32 v48, v48, v64\n" \
  "v_mul_f32 v49, v49, v65\n" \
  "v_mul_f32 v50, v50, v66\n" \
  "v_mul_f32 v51, v51, v67\n" \
  "v_mul_f32 v52, v52, v68\n" \
  "v_mul_f32 v53, v53, v69\n" \
  "s_waitcnt lgkmcnt(3)\n" \
  "v_mfma_f32_32x32x16_bf16 v[80:95], v[20:23], v[40:43], v[80:95]\n" \
  "v_mul_f32 v54, v54, v70\n" \
  "v_mul_f32 v55, v55, v71\n" \
  "v_mul_f32 v56, v56, v72\n" \
  "v_mul_f32 v57, v57, v73\n" \
  "v_mul_f32 v58, v58, v74\n" \
  "v_mul_f32 v59, v59, v75\n" \
  "s_waitcnt lgkmcnt(2)\n" \
  "v_mfma_f32_32x32x16_bf16 v[80:95], v[24:27], v[44:47], v[80:95]\n" \
  "v_mul_f32 v60, v60, v76\n" \
  "v_mul_f32 v61, v61, v77\n" \
  "v_mul_f32 v62, v62, v78\n" \
  "v_mul_f32 v63, v63, v79\n" \
  "s_waitcnt lgkmcnt(1)\n" \
  "v_mfma_f32_32x32x16_bf16 v[80:95], v[28:31], v[32:35], v[80:95]\n" \
  "s_waitcnt lgkmcnt(0)\n" \
  "v_mfma_f32_32x32x16_bf16 v[64:79], v[12:15], v[36:39], 0\n" \
  "s_setprio 0\n" \
  "buffer_load_dwordx4 v[12:15], v124, %[rtab], 0 offen\n" \
  "s_waitcnt vmcnt(5)\n" \
  "ds_read_b64_tr_b16 v[104:105], v3 offset:0\n" \
  "ds_read_b64_tr_b16 v[106:107], v9 offset:256\n" \
  "ds_read_b64_tr_b16 v[108:109], v10 offset:1024\n" \
  "ds_read_b64_tr_b16 v[110:111], v11 offset:1280\n" \
  "ds_read_b64_tr_b16 v[112:113], v3 offset:2048\n" \
  "ds_read_b64_tr_b16 v[114:115], v9 offset:2304\n" \
  "ds_read_b64_tr_b16 v[116:117], v10 offset:3072\n" \
  "ds_read_b64_tr_b16 v[118:119], v11 offset:3328\n" \
  "ds_read_b64_tr_b16 v[16:17], v9 offset:0\n" \
  "ds_read_b64_tr_b16 v[18:19], v3 offset:256\n" \
  "ds_read_b64_tr_b16 v[20:21], v11 offset:1024\n" \
  "ds_read_b64_tr_b16 v[22:23], v10 offset:1280\n" \
  "ds_read_b64_tr_b16 v[24:25], v9 offset:2048\n" \
  "ds_read_b64_tr_b16 v[26:27], v3 offset:2304\n" \
  "ds_read_b64_tr_b16 v[28:29], v11 offset:3072\n" \
  "s_waitcnt lgkmcnt(14)\n" \
  "ds_read_b64_tr_b16 v[30:31], v10 offset:3328\n" \
  "v_mul_f32 v80, v80, v64\n" \
  "v_mul_f32 v81, v81, v65\n" \
  "v_mul_f32 v82, v82, v66\n" \
  "v_mul_f32 v83, v83, v67\n" \
  "v_mul_f32 v84, v84, v68\n" \
  "v_mul_f32 v85, v85, v69\n" \
  "v_mul_f32 v86, v86, v70\n" \
  "v_mul_f32 v87, v87, v71\n" \
  "v_mul_f32 v88, v88, v72\n" \
  "v_mul_f32 v89, v89, v73\n" \
  "v_mul_f32 v90, v90, v74\n" \
  "v_mul_f32 v91, v91, v75\n" \
  "v_mul_f32 v92, v92, v76\n" \
  "v_mul_f32 v93, v93, v77\n" \
  "v_mul_f32 v94, v94, v78\n" \
  "v_mul_f32 v95, v95, v79\n" \
  "s_waitcnt lgkmcnt(14)\n" \
  "ds_read_b64_tr_b16 v[32:33], v3 offset:4096\n" \
  "s_waitcnt lgkmcnt(14)\n" \
  "ds_read_b64_tr_b16 v[34:35], v9 offset:4352\n" \
  "s_waitcnt lgkmcnt(14)\n" \
  "ds_read_b64_tr_b16 v[36:37], v10 offset:5120\n" \
  "s_waitcnt lgkmcnt(14)\n" \
  "ds_read_b64_tr_b16 v[38:39], v11 offset:5376\n" \
  "s_waitcnt lgkmcnt(14)\n" \
  "ds_read_b64_tr_b16 v[40:41], v3 offset:6144\n" \
  "s_waitcnt lgkmcnt(14)\n" \
  "ds_read_b64_tr_b16 v[42:43], v9 offset:6400\n" \
  "s_waitcnt lgkmcnt(14)\n" \
  "ds_read_b64_tr_b16 v[44:45], v10 offset:7168\n" \
  "s_waitcnt lgkmcnt(14)\n" \
  "ds_read_b64_tr_b16 v[46:47], v11 offset:7424\n" \
  "v_mul_f32 v104, v104, v48\n" \
  "v_mul_f32 v105, v105, v49\n" \
  "s_waitcnt lgkmcnt(8)\n" \
  "s_add_u32 m0, %[buf], 3968\n" \
  "v_mul_f32 v106, v106, v50\n" \
  "buffer_load_dwordx4 v120, %[rg], 0 offen offset:128 lds\n" \
  "buffer_load_dwordx4 v121, %[rg], 0 offen offset:1152 lds\n" \
  "v_mul_f32 v107, v107, v51\n" \
  "buffer_load_dwordx4 v122, %[rg], 0 offen offset:2176 lds\n" \
  "buffer_load_dwordx4 v123, %[rg], 0 offen offset:3200 lds\n" \
  "v_mul_f32 v108, v108, v52\n" \
  "ds_read_b64_tr_b16 v[64:65], v9 offset:4096\n" \
  "ds_read_b64_tr_b16 v[66:67], v3 offset:4352\n" \
  "v_mul_f32 v109, v109, v53\n" \
  "ds_read_b64_tr_b16 v[68:69], v11 offset:5120\n" \
  "ds_read_b64_tr_b16 v[70:71], v10 offset:5376\n" \
  "v_mul_f32 v110, v110, v54\n" \
  "ds_read_b64_tr_b16 v[72:73], v9 offset:6144\n" \
  "ds_read_b64_tr_b16 v[74:75], v3 offset:6400\n" \
  "v_mul_f32 v111, v111, v55\n" \
  "ds_read_b64_tr_b16 v[76:77], v11 offset:7168\n" \
  "s_waitcnt lgkmcnt(14)\n" \
  "ds_read_b64_tr_b16 v[78:79], v10 offset:7424\n" \
  "v_cvt_pk_bf16_f32 v104, v104, v105\n" \
  "v_cvt_pk_bf16_f32 v105, v106, v107\n" \
  "v_cvt_pk_bf16_f32 v106, v108, v109\n" \
  "v_cvt_pk_bf16_f32 v107, v110, v111\n" \
  "v_mul_f32 v112, v112, v56\n" \
  "v_mul_f32 v113, v113, v57\n" \
  "v_mfma_f32_32x32x16_bf16 v[160:175], v[104:107], v[96:99], v[160:175]\n" \
  "v_mul_f32 v114, v114, v58\n" \
  "v_mul_f32 v115, v115, v59\n" \
  "v_mul_f32 v116, v116, v60\n" \
  "v_mul_f32 v117, v117, v61\n" \
  "v_mul_f32 v118, v118, v62\n" \
  "v_mul_f32 v119, v119, v63\n" \
  "v_cvt_pk_bf16_f32 v108, v112, v113\n" \
  "v_cvt_pk_bf16_f32 v109, v114, v115\n" \
  "v_cvt_pk_bf16_f32 v110, v116, v117\n" \
  "v_cvt_pk_bf16_f32 v111, v118, v119\n" \
  "v_mul_f32 v16, v16, v80\n" \
  "v_mul_f32 v17, v17, v81\n" \
  "v_mfma_f32_32x32x16_bf16 v[160:175], v[108:111], v[100:103], v[160:175]\n" \
  "s_waitcnt lgkmcnt(14)\n" \
  "ds_read_b64_tr_b16 v[104:105], v3 offset:8192\n" \
  "s_waitcnt lgkmcnt(14)\n" \
  "ds_read_b64_tr_b16 v[106:107], v9 offset:8448\n" \
  "v_mul_f32 v18, v18, v82\n" \
  "s_waitcnt lgkmcnt(14)\n" \
  "ds_read_b64_tr_b16 v[108:109], v10 offset:9216\n" \
  "s_waitcnt lgkmcnt(14)\n" \
  "ds_read_b64_tr_b16 v[110:111], v11 offset:9472\n" \
  "v_mul_f32 v19, v19, v83\n" \
  "s_waitcnt lgkmcnt(14)\n" \
  "ds_read_b64_tr_b16 v[112:113], v3 offset:10240\n" \
  "s_waitcnt lgkmcnt(14)\n" \
  "ds_read_b64_tr_b16 v[114:115], v9 offset:10496\n" \
  "v_mul_f32 v20, v20, v84\n" \
  "s_waitcnt lgkmcnt(14)\n" \
  "ds_read_b64_tr_b16 v[116:117], v10 offset:11264\n" \
  "s_waitcnt lgkmcnt(14)\n" \
  "ds_read_b64_tr_b16 v[118:119], v11 offset:11520\n" \
  "v_mul_f32 v21, v21, v85\n" \
  "v_mul_f32 v22, v22, v86\n" \
  "v_mul_f32 v23, v23, v87\n" \
  "v_cvt_pk_bf16_f32 v16, v16, v17\n" \
  "v_cvt_pk_bf16_f32 v17, v18, v19\n" \
  "v_cvt_pk_bf16_f32 v18, v20, v21\n" \
  "v_cvt_pk_bf16_f32 v19, v22, v23\n" \
  "v_mul_f32 v24, v24, v88\n" \
  "v_mul_f32 v25, v25, v89\n" \
  "v_mfma_f32_32x32x16_bf16 v[176:191], v[16:19], v[96:99], v[176:191]\n" \
  "v_mul_f32 v26, v26, v90\n" \
  "v_mul_f32 v27, v27, v91\n" \
  "v_mul_f32 v28, v28, v92\n" \
  "v_mul_f32 v29, v29, v93\n" \
  "v_mul_f32 v30, v30, v94\n" \
  "v_mul_f32 v31, v31, v95\n" \
  "v_cvt_pk_bf16_f32 v20, v24, v25\n" \
  "v_cvt_pk_bf16_f32 v21, v26, v27\n" \
  "v_cvt_pk_bf16_f32 v22, v28, v29\n" \
  "v_cvt_pk_bf16_f32 v23, v30, v31\n" \
  "v_mul_f32 v32, v32, v48\n" \
  "v_mul_f32 v33, v33, v49\n" \
  "v_mfma_f32_32x32x16_bf16 v[176:191], v[20:23], v[100:103], v[176:191]\n" \
  "s_waitcnt lgkmcnt(8)\n" \
  "s_add_u32 m0, %[buf], 7936\n" \
  "v_mul_f32 v34, v34, v50\n" \
  "buffer_load_dwordx4 v120, %[rg], 0 offen offset:256 lds\n" \
  "buffer_load_dwordx4 v121, %[rg], 0 offen offset:1280 lds\n" \
  "v_mul_f32 v35, v35, v51\n" \
  "buffer_load_dwordx4 v122, %[rg], 0 offen offset:2304 lds\n" \
  "buffer_load_dwordx4 v123, %[rg], 0 offen offset:3328 lds\n" \
  "v_mul_f32 v36, v36, v52\n" \
  "ds_read_b64_tr_b16 v[16:17], v9 offset:8192\n" \
  "ds_read_b64_tr_b16 v[18:19], v3 offset:8448\n" \
  "v_mul_f32 v37, v37, v53\n" \
  "ds_read_b64_tr_b16 v[20:21], v11 offset:9216\n" \
  "ds_read_b64_tr_b16 v[22:23], v10 offset:9472\n" \
  "v_mul_f32 v38, v38, v54\n" \
  "ds_read_b64_tr_b16 v[24:25], v9 offset:10240\n" \
  "ds_read_b64_tr_b16 v[26:27], v3 offset:10496\n" \
  "v_mul_f32 v39, v39, v55\n" \
  "ds_read_b64_tr_b16 v[28:29], v11 offset:11264\n" \
  "s_waitcnt lgkmcnt(14)\n" \
  "ds_read_b64_tr_b16 v[30:31], v10 offset:11520\n" \
  "v_cvt_pk_bf16_f32 v32, v32, v33\n" \
  "v_cvt_pk_bf16_f32 v33, v34, v35\n" \
  "v_cvt_pk_bf16_f32 v34, v36, v37\n" \
  "v_cvt_pk_bf16_f32 v35, v38, v39\n" \
  "v_mul_f32 v40, v40, v56\n" \
  "v_mul_f32 v41, v41, v57\n" \
  "v_mfma_f32_32x32x16_bf16 v[192:207], v[32:35], v[96:99], v[192:207]\n" \
  "v_mul_f32 v42, v42, v58\n" \
  "v_mul_f32 v43, v43, v59\n" \
  "v_mul_f32 v44, v44, v60\n" \
  "v_mul_f32 v45, v45, v61\n" \
  "v_mul_f32 v46, v46, v62\n" \
  "v_mul_f32 v47, v47, v63\n" \
  "v_cvt_pk_bf16_f32 v36, v40, v41\n" \
  "v_cvt_pk_bf16_f32 v37, v42, v43\n" \
  "v_cvt_pk_bf16_f32 v38, v44, v45\n" \
  "v_cvt_pk_bf16_f32 v39, v46, v47\n" \
  "v_mul_f32 v64, v64, v80\n" \
  "v_mul_f32 v65, v65, v81\n" \
  "v_mfma_f32_32x32x16_bf16 v[192:207], v[36:39], v[100:103], v[192:207]\n" \
  "v_mul_f32 v66, v66, v82\n" \
  "v_mul_f32 v67, v67, v83\n" \
  "v_mul_f32 v68, v68, v84\n" \
  "v_mul_f32 v69, v69, v85\n" \
  "v_mul_f32 v70, v70, v86\n" \
  "v_mul_f32 v71, v71, v87\n" \
  "v_cvt_pk_bf16_f32 v64, v64, v65\n" \
  "v_cvt_pk_bf16_f32 v65, v66, v67\n" \
  "v_cvt_pk_bf16_f32 v66, v68, v69\n" \
  "v_cvt_pk_bf16_f32 v67, v70, v71\n" \
  "v_mul_f32 v72, v72, v88\n" \
  "v_mul_f32 v73, v73, v89\n" \
  "v_mfma_f32_32x32x16_bf16 v[208:223], v[64:67], v[96:99], v[208:223]\n" \
  "v_mul_f32 v74, v74, v90\n" \
  "v_mul_f32 v75, v75, v91\n" \
  "v_mul_f32 v76, v76, v92\n" \
  "v_mul_f32 v77, v77, v93\n" \
  "v_mul_f32 v78, v78, v94\n" \
  "v_mul_f32 v79, v79, v95\n" \
  "v_cvt_pk_bf16_f32 v68, v72, v73\n" \
  "v_cvt_pk_bf16_f32 v69, v74, v75\n" \
  "v_cvt_pk_bf16_f32 v70, v76, v77\n" \
  "v_cvt_pk_bf16_f32 v71, v78, v79\n" \
  "v_mul_f32 v104, v104, v48\n" \
  "v_mul_f32 v105, v105, v49\n" \
  "v_mfma_f32_32x32x16_bf16 v[208:223], v[68:71], v[100:103], v[208:223]\n" \
  "s_waitcnt lgkmcnt(0)\n" \
  "s_add_u32 m0, %[buf], 11904\n" \
  "v_mul_f32 v106, v106, v50\n" \
  "buffer_load_dwordx4 v120, %[rg], 0 offen offset:384 lds\n" \
  "buffer_load_dwordx4 v121, %[rg], 0 offen offset:1408 lds\n" \
  "v_mul_f32 v107, v107, v51\n" \
  "buffer_load_dwordx4 v122, %[rg], 0 offen offset:2432 lds\n" \
  "buffer_load_dwordx4 v123, %[rg], 0 offen offset:3456 lds\n" \
  "v_mul_f32 v108, v108, v52\n" \
  "v_mul_f32 v109, v109, v53\n" \
  "v_mul_f32 v110, v110, v54\n" \
  "v_mul_f32 v111, v111, v55\n" \
  "v_cvt_pk_bf16_f32 v104, v104, v105\n" \
  "v_cvt_pk_bf16_f32 v105, v106, v107\n" \
  "v_cvt_pk_bf16_f32 v106, v108, v109\n" \
  "v_cvt_pk_bf16_f32 v107, v110, v111\n" \
  "v_mul_f32 v112, v112, v56\n" \
  "v_mul_f32 v113, v113, v57\n" \
  "v_mfma_f32_32x32x16_bf16 v[224:239], v[104:107], v[96:99], v[224:239]\n" \
  "v_mul_f32 v114, v114, v58\n" \
  "v_mul_f32 v115, v115, v59\n" \
  "v_mul_f32 v116, v116, v60\n" \
  "v_mul_f32 v117, v117, v61\n" \
  "v_mul_f32 v118, v118, v62\n" \
  "v_mul_f32 v119, v119, v63\n" \
  "v_cvt_pk_bf16_f32 v108, v112, v113\n" \
  "v_cvt_pk_bf16_f32 v109, v114, v115\n" \
  "v_cvt_pk_bf16_f32 v110, v116, v117\n" \
  "v_cvt_pk_bf16_f32 v111, v118, v119\n" \
  "v_mul_f32 v16, v16, v80\n" \
  "v_mul_f32 v17, v17, v81\n" \
  "v_mfma_f32_32x32x16_bf16 v[224:239], v[108:111], v[100:103], v[224:239]\n" \
  "v_mul_f32 v18, v18, v82\n" \
  "v_mul_f32 v19, v19, v83\n" \
  "v_mul_f32 v20, v20, v84\n" \
  "v_mul_f32 v21, v21, v85\n" \
  "v_mul_f32 v22, v22, v86\n" \
  "v_mul_f32 v23, v23, v87\n" \
  "v_cvt_pk_bf16_f32 v16, v16, v17\n" \
  "v_cvt_pk_bf16_f32 v17, v18, v19\n" \
  "v_cvt_pk_bf16_f32 v18, v20, v21\n" \
  "v_cvt_pk_bf16_f32 v19, v22, v23\n" \
  "v_mul_f32 v24, v24, v88\n" \
  "v_mul_f32 v25, v25, v89\n" \
  "v_mfma_f32_32x32x16_bf16 v[240:255], v[16:19], v[96:99], v[240:255]\n" \
  "v_mul_f32 v26, v26, v90\n" \
  "v_mul_f32 v27, v27, v91\n" \
  "v_mul_f32 v28, v28, v92\n" \
  "v_mul_f32 v29, v29, v93\n" \
  "v_mul_f32 v30, v30, v94\n" \
  "v_mul_f32 v31, v31, v95\n" \
  "v_cvt_pk_bf16_f32 v20, v24, v25\n" \
  "v_cvt_pk_bf16_f32 v21, v26, v27\n" \
  "v_cvt_pk_bf16_f32 v22, v28, v29\n" \
  "v_cvt_pk_bf16_f32 v23, v30, v31\n" \
  "v_add_u32 v5, 384, v5\n" \
  "v_add_u32 v6, 128, v6\n" \
  "v_add_u32 v7, 384, v7\n" \
  "v_mfma_f32_32x32x16_bf16 v[240:255], v[20:23], v[100:103], v[240:255]\n" \
  "s_sub_u32 %[nt], %[nt], 1\n" \
  "s_cmp_gt_u32 %[nt], 1\n" \
  "s_cbranch_scc1 .Lloop%=\n" \
  ".Llast%=:\n" \
  "s_cmp_lg_u32 %[half], 0\n" \
  "s_cbranch_scc1 .Lhalf%=\n" \
  "s_setprio 1\n" \
  "ds_read_b128 v[80:83], v5 offset:0\n" \
  "ds_read_b128 v[84:87], v5 offset:32\n" \
  "v_xor_b32 v9, 16, v1\n" \
  "v_xor_b32 v10, 32, v1\n" \
  "v_xor_b32 v11, 48, v1\n" \
  "s_waitcnt vmcnt(12)\n" \
  "ds_read_b128 v[16:19], v1 offset:0\n" \
  "ds_read_b128 v[32:35], v2 offset:16384\n" \
  "ds_read_b128 v[20:23], v9 offset:0\n" \
  "ds_read_b128 v[36:39], v2 offset:17408\n" \
  "ds_read_b128 v[24:27], v10 offset:0\n" \
  "ds_read_b128 v[40:43], v2 offset:18432\n" \
  "ds_read_b128 v[28:31], v11 offset:0\n" \
  "ds_read_b128 v[44:47], v2 offset:19456\n" \
  "ds_read_b128 v[120:123], v5 offset:128\n" \
  "ds_read_b128 v[124:127], v5 offset:160\n" \
  "s_waitcnt lgkmcnt(11)\n" \
  "v_xor_b32 v96, v80, v4\n" \
  "v_pk_sub_u16 v96, 1, v96 op_sel_hi:[0,1] clamp\n" \
  "v_xor_b32 v97, v81, v4\n" \
  "v_pk_sub_u16 v97, 1, v97 op_sel_hi:[0,1] clamp\n" \
  "v_xor_b32 v98, v82, v4\n" \
  "v_pk_sub_u16 v98, 1, v98 op_sel_hi:[0,1] clamp\n" \
  "v_xor_b32 v99, v83, v4\n" \
  "v_pk_sub_u16 v99, 1, v99 op_sel_hi:[0,1] clamp\n" \
  "s_waitcnt lgkmcnt(8)\n" \
  "v_mfma_f32_32x32x16_bf16 v[48:63], v[16:19], v[32:35], 0\n" \
  "s_cbranch_vccz .Lfl2%=\n" \
  "s_add_u32 m0, %[buf], 7936\n" \
  "s_nop 0\n" \
  "buffer_load_dwordx4 v112, %[rg], 0 offen offset:256 lds\n" \
  "buffer_load_dwordx4 v113, %[rg], 0 offen offset:1280 lds\n" \
  "buffer_load_dwordx4 v114, %[rg], 0 offen offset:2304 lds\n" \
  "buffer_load_dwordx4 v115, %[rg], 0 offen offset:3328 lds\n" \
  ".Lfl2%=:\n" \
  "ds_read_b128 v[32:35], v2 offset:28672\n" \
  "v_xor_b32 v100, v84, v4\n" \
  "v_pk_sub_u16 v100, 1, v100 op_sel_hi:[0,1] clamp\n" \
  "v_xor_b32 v101, v85, v4\n" \
  "v_pk_sub_u16 v101, 1, v101 op_sel_hi:[0,1] clamp\n" \
  "v_xor_b32 v102, v86, v4\n" \
  "v_pk_sub_u16 v102, 1, v102 op_sel_hi:[0,1] clamp\n" \
  "s_waitcnt lgkmcnt(7)\n" \
  "v_mfma_f32_32x32x16_bf16 v[48:63], v[20:23], v[36:39], v[48:63]\n" \
  "s_cbranch_vccz .Lfl3%=\n" \
  "s_add_u32 m0, %[buf], 11904\n" \
  "s_nop 0\n" \
  "buffer_load_dwordx4 v112, %[rg], 0 offen offset:384 lds\n" \
  "buffer_load_dwordx4 v113, %[rg], 0 offen offset:1408 lds\n" \
  "buffer_load_dwordx4 v114, %[rg], 0 offen offset:2432 lds\n" \
  "buffer_load_dwordx4 v115, %[rg], 0 offen offset:3456 lds\n" \
  ".Lfl3%=:\n" \
  "ds_read_b128 v[36:39], v2 offset:20480\n" \
  "v_xor_b32 v103, v87, v4\n" \
  "v_pk_sub_u16 v103, 1, v103 op_sel_hi:[0,1] clamp\n" \
  "s_waitcnt lgkmcnt(3)\n" \
  "v_pk_mul_lo_u16 v120, v96, v120\n" \
  "v_pk_mul_lo_u16 v121, v97, v121\n" \
  "v_pk_mul_lo_u16 v122, v98, v122\n" \
  "v_pk_mul_lo_u16 v123, v99, v123\n" \
  "v_mfma_f32_32x32x16_bf16 v[48:63], v[24:27], v[40:43], v[48:63]\n" \
  "ds_read_b128 v[40:43], v2 offset:21504\n" \
  "s_waitcnt lgkmcnt(3)\n" \
  "v_pk_mul_lo_u16 v124, v100, v124\n" \
  "v_pk_mul_lo_u16 v125, v101, v125\n" \
  "v_pk_mul_lo_u16 v126, v102, v126\n" \
  "v_pk_mul_lo_u16 v127, v103, v127\n" \
  "v_mfma_f32_32x32x16_bf16 v[48:63], v[28:31], v[44:47], v[48:63]\n" \
  "ds_read_b128 v[44:47], v2 offset:22528\n" \
  "s_waitcnt lgkmcnt(3)\n" \
  "v_mfma_f32_32x32x16_bf16 v[64:79], v[12:15], v[32:35], 0\n" \
  "ds_read_b128 v[32:35], v2 offset:23552\n" \
  "s_mov_b64 vcc, 0\n" \
  "s_waitcnt lgkmcnt(3)\n" \
  "v_mfma_f32_32x32x16_bf16 v[80:95], v[16:19], v[36:39], 0\n" \
  "ds_read_b128 v[36:39], v2 offset:29696\n" \
  "s_waitcnt lgkmcnt(3)\n" \
  "v_mfma_f32_32x32x16_bf16 v[80:95], v[20:23], v[40:43], v[80:95]\n" \
  "ds_read_b128 v[40:43], v2 offset:0\n" \
  "s_nop 3\n" \
  "v_mul_f32 v48, v48, v64\n" \
  "v_mul_f32 v49, v49, v65\n" \
  "v_mul_f32 v50, v50, v66\n" \
  "v_mul_f32 v51, v51, v67\n" \
  "v_mul_f32 v52, v52, v68\n" \
  "v_mul_f32 v53, v53, v69\n" \
  "s_waitcnt lgkmcnt(3)\n" \
  "v_mfma_f32_32x32x16_bf16 v[80:95], v[24:27], v[44:47], v[80:95]\n" \
  "ds_read_b128 v[44:47], v2 offset:1024\n" \
  "v_mul_f32 v54, v54, v70\n" \
  "v_mul_f32 v55, v55, v71\n" \
  "v_mul_f32 v56, v56, v72\n" \
  "v_mul_f32 v57, v57, v73\n" \
  "v_mul_f32 v58, v58, v74\n" \
  "v_mul_f32 v59, v59, v75\n" \
  "s_waitcnt lgkmcnt(3)\n" \
  "v_mfma_f32_32x32x16_bf16 v[80:95], v[28:31], v[32:35], v[80:95]\n" \
  "ds_read_b128 v[32:35], v2 offset:2048\n" \
  "v_mul_f32 v60, v60, v76\n" \
  "v_mul_f32 v61, v61, v77\n" \
  "v_mul_f32 v62, v62, v78\n" \
  "v_mul_f32 v63, v63, v79\n" \
  "s_waitcnt lgkmcnt(3)\n" \
  "v_mfma_f32_32x32x16_bf16 v[64:79], v[12:15], v[36:39], 0\n" \
  "ds_read_b128 v[36:39], v2 offset:3072\n" \
  "v_cvt_pk_bf16_f32 v104, v48, v49\n" \
  "v_cvt_pk_bf16_f32 v105, v50, v51\n" \
  "v_cvt_pk_bf16_f32 v106, v52, v53\n" \
  "v_cvt_pk_bf16_f32 v107, v54, v55\n" \
  "v_cvt_pk_bf16_f32 v108, v56, v57\n" \
  "v_cvt_pk_bf16_f32 v109, v58, v59\n" \
  "v_cvt_pk_bf16_f32 v110, v60, v61\n" \
  "v_cvt_pk_bf16_f32 v111, v62, v63\n" \
  "s_waitcnt lgkmcnt(3)\n" \
  "v_mfma_f32_32x32x16_bf16 v[48:63], v[16:19], v[40:43], 0\n" \
  "ds_read_b128 v[40:43], v2 offset:24576\n" \
  "s_waitcnt lgkmcnt(3)\n" \
  "v_mfma_f32_32x32x16_bf16 v[48:63], v[20:23], v[44:47], v[48:63]\n" \
  "ds_read_b128 v[44:47], v2 offset:4096\n" \
  "v_mul_f32 v80, v80, v64\n" \
  "v_mul_f32 v81, v81, v65\n" \
  "v_mul_f32 v82, v82, v66\n" \
  "v_mul_f32 v83, v83, v67\n" \
  "v_mul_f32 v84, v84, v68\n" \
  "v_mul_f32 v85, v85, v69\n" \
  "s_waitcnt lgkmcnt(3)\n" \
  "v_mfma_f32_32x32x16_bf16 v[48:63], v[24:27], v[32:35], v[48:63]\n" \
  "ds_read_b128 v[32:35], v2 offset:5120\n" \
  "v_mul_f32 v86, v86, v70\n" \
  "v_mul_f32 v87, v87, v71\n" \
  "v_mul_f32 v88, v88, v72\n" \
  "v_mul_f32 v89, v89, v73\n" \
  "v_mul_f32 v90, v90, v74\n" \
  "v_mul_f32 v91, v91, v75\n" \
  "s_waitcnt lgkmcnt(3)\n" \
  "v_mfma_f32_32x32x16_bf16 v[48:63], v[28:31], v[36:39], v[48:63]\n" \
  "ds_read_b128 v[36:39], v2 offset:6144\n" \
  "v_mul_f32 v92, v92, v76\n" \
  "v_mul_f32 v93, v93, v77\n" \
  "v_mul_f32 v94, v94, v78\n" \
  "v_mul_f32 v95, v95, v79\n" \
  "s_waitcnt lgkmcnt(3)\n" \
  "v_mfma_f32_32x32x16_bf16 v[64:79], v[12:15], v[40:43], 0\n" \
  "ds_read_b128 v[40:43], v2 offset:7168\n" \
  "v_cvt_pk_bf16_f32 v112, v80, v81\n" \
  "v_cvt_pk_bf16_f32 v113, v82, v83\n" \
  "v_cvt_pk_bf16_f32 v114, v84, v85\n" \
  "v_cvt_pk_bf16_f32 v115, v86, v87\n" \
  "v_cvt_pk_bf16_f32 v116, v88, v89\n" \
  "v_cvt_pk_bf16_f32 v117, v90, v91\n" \
  "v_cvt_pk_bf16_f32 v118, v92, v93\n" \
  "v_cvt_pk_bf16_f32 v119, v94, v95\n" \
  "ds_read_b128 v[80:83], v5 offset:192\n" \
  "ds_read_b128 v[84:87], v5 offset:224\n" \
  "ds_read_b128 v[88:91], v5 offset:256\n" \
  "ds_read_b128 v[92:95], v5 offset:288\n" \
  "v_mfma_f32_32x32x16_bf16 v[160:175], v[104:107], v[120:123], v[160:175]\n" \
  "v_mfma_f32_32x32x16_bf16 v[176:191], v[112:115], v[120:123], v[176:191]\n" \
  "s_waitcnt lgkmcnt(3)\n" \
  "v_pk_mul_lo_u16 v120, v96, v80\n" \
  "v_pk_mul_lo_u16 v121, v97, v81\n" \
  "v_pk_mul_lo_u16 v122, v98, v82\n" \
  "v_pk_mul_lo_u16 v123, v99, v83\n" \
  "v_mfma_f32_32x32x16_bf16 v[160:175], v[108:111], v[124:127], v[160:175]\n" \
  "v_mul_f32 v48, v48, v64\n" \
  "v_mul_f32 v49, v49, v65\n" \
  "v_mul_f32 v50, v50, v66\n" \
  "v_mul_f32 v51, v51, v67\n" \
  "v_mul_f32 v52, v52, v68\n" \
  "v_mul_f32 v53, v53, v69\n" \
  "v_mfma_f32_32x32x16_bf16 v[176:191], v[116:119], v[124:127], v[176:191]\n" \
  "s_waitcnt lgkmcnt(2)\n" \
  "v_pk_mul_lo_u16 v124, v100, v84\n" \
  "v_pk_mul_lo_u16 v125, v101, v85\n" \
  "v_pk_mul_lo_u16 v126, v102, v86\n" \
  "v_pk_mul_lo_u16 v127, v103, v87\n" \
  "v_mul_f32 v54, v54, v70\n" \
  "v_mul_f32 v55, v55, v71\n" \
  "v_mfma_f32_32x32x16_bf16 v[192:207], v[104:107], v[120:123], v[192:207]\n" \
  "v_mul_f32 v56, v56, v72\n" \
  "v_mul_f32 v57, v57, v73\n" \
  "v_mul_f32 v58, v58, v74\n" \
  "v_mul_f32 v59, v59, v75\n" \
  "v_mul_f32 v60, v60, v76\n" \
  "v_mul_f32 v61, v61, v77\n" \
  "v_mfma_f32_32x32x16_bf16 v[208:223], v[112:115], v[120:123], v[208:223]\n" \
  "s_waitcnt lgkmcnt(1)\n" \
  "v_pk_mul_lo_u16 v120, v96, v88\n" \
  "v_pk_mul_lo_u16 v121, v97, v89\n" \
  "v_pk_mul_lo_u16 v122, v98, v90\n" \
  "v_pk_mul_lo_u16 v123, v99, v91\n" \
  "v_mul_f32 v62, v62, v78\n" \
  "v_mul_f32 v63, v63, v79\n" \
  "v_mfma_f32_32x32x16_bf16 v[192:207], v[108:111], v[124:127], v[192:207]\n" \
  "v_cvt_pk_bf16_f32 v48, v48, v49\n" \
  "v_cvt_pk_bf16_f32 v49, v50, v51\n" \
  "v_cvt_pk_bf16_f32 v50, v52, v53\n" \
  "v_cvt_pk_bf16_f32 v51, v54, v55\n" \
  "v_cvt_pk_bf16_f32 v52, v56, v57\n" \
  "v_cvt_pk_bf16_f32 v53, v58, v59\n" \
  "v_mfma_f32_32x32x16_bf16 v[208:223], v[116:119], v[124:127], v[208:223]\n" \
  "s_waitcnt lgkmcnt(0)\n" \
  "v_pk_mul_lo_u16 v124, v100, v92\n" \
  "v_pk_mul_lo_u16 v125, v101, v93\n" \
  "v_pk_mul_lo_u16 v126, v102, v94\n" \
  "v_pk_mul_lo_u16 v127, v103, v95\n" \
  "v_cvt_pk_bf16_f32 v54, v60, v61\n" \
  "v_cvt_pk_bf16_f32 v55, v62, v63\n" \
  "v_mfma_f32_32x32x16_bf16 v[224:239], v[104:107], v[120:123], v[224:239]\n" \
  "v_mfma_f32_32x32x16_bf16 v[240:255], v[112:115], v[120:123], v[240:255]\n" \
  "v_mfma_f32_32x32x16_bf16 v[224:239], v[108:111], v[124:127], v[224:239]\n" \
  "v_mfma_f32_32x32x16_bf16 v[240:255], v[116:119], v[124:127], v[240:255]\n" \
  "ds_read_b128 v[120:123], v5 offset:64\n" \
  "ds_read_b128 v[124:127], v5 offset:96\n" \
  "v_mfma_f32_32x32x16_bf16 v[80:95], v[16:19], v[44:47], 0\n" \
  "ds_read_b128 v[44:47], v2 offset:25600\n" \
  "v_mfma_f32_32x32x16_bf16 v[80:95], v[20:23], v[32:35], v[80:95]\n" \
  "ds_read_b128 v[32:35], v2 offset:8192\n" \
  "v_mfma_f32_32x32x16_bf16 v[80:95], v[24:27], v[36:39], v[80:95]\n" \
  "ds_read_b128 v[36:39], v2 offset:9216\n" \
  "s_waitcnt lgkmcnt(4)\n" \
  "v_pk_mul_lo_u16 v96, v96, v120\n" \
  "v_pk_mul_lo_u16 v97, v97, v121\n" \
  "v_pk_mul_lo_u16 v98, v98, v122\n" \
  "v_pk_mul_lo_u16 v99, v99, v123\n" \
  "s_waitcnt lgkmcnt(3)\n" \
  "v_pk_mul_lo_u16 v100, v100, v124\n" \
  "v_pk_mul_lo_u16 v101, v101, v125\n" \
  "v_mfma_f32_32x32x16_bf16 v[80:95], v[28:31], v[40:43], v[80:95]\n" \
  "ds_read_b128 v[40:43], v2 offset:10240\n" \
  "v_pk_mul_lo_u16 v102, v102, v126\n" \
  "v_pk_mul_lo_u16 v103, v103, v127\n" \
  "s_waitcnt lgkmcnt(3)\n" \
  "v_mfma_f32_32x32x16_bf16 v[64:79], v[12:15], v[44:47], 0\n" \
  "ds_read_b128 v[44:47], v2 offset:11264\n" \
  "v_mfma_f32_32x32x16_bf16 v[128:143], v[48:51], v[96:99], v[128:143]\n" \
  "v_mfma_f32_32x32x16_bf16 v[128:143], v[52:55], v[100:103], v[128:143]\n" \
  "s_waitcnt lgkmcnt(3)\n" \
  "v_mfma_f32_32x32x16_bf16 v[48:63], v[16:19], v[32:35], 0\n" \
  "ds_read_b128 v[32:35], v2 offset:26624\n" \
  "s_nop 5\n" \
  "v_mul_f32 v80, v80, v64\n" \
  "v_mul_f32 v81, v81, v65\n" \
  "v_mul_f32 v82, v82, v66\n" \
  "v_mul_f32 v83, v83, v67\n" \
  "v_mul_f32 v84, v84, v68\n" \
  "v_mul_f32 v85, v85, v69\n" \
  "s_waitcnt lgkmcnt(3)\n" \
  "v_mfma_f32_32x32x16_bf16 v[48:63], v[20:23], v[36:39], v[48:63]\n" \
  "ds_read_b128 v[36:39], v2 offset:12288\n" \
  "v_mul_f32 v86, v86, v70\n" \
  "v_mul_f32 v87, v87, v71\n" \
  "v_mul_f32 v88, v88, v72\n" \
  "v_mul_f32 v89, v89, v73\n" \
  "v_mul_f32 v90, v90, v74\n" \
  "v_mul_f32 v91, v91, v75\n" \
  "s_waitcnt lgkmcnt(3)\n" \
  "v_mfma_f32_32x32x16_bf16 v[48:63], v[24:27], v[40:43], v[48:63]\n" \
  "ds_read_b128 v[40:43], v2 offset:13312\n" \
  "v_mul_f32 v92, v92, v76\n" \
  "v_mul_f32 v93, v93, v77\n" \
  "v_mul_f32 v94, v94, v78\n" \
  "v_mul_f32 v95, v95, v79\n" \
  "s_waitcnt lgkmcnt(3)\n" \
  "v_mfma_f32_32x32x16_bf16 v[48:63], v[28:31], v[44:47], v[48:63]\n" \
  "ds_read_b128 v[44:47], v2 offset:14336\n" \
  "s_waitcnt lgkmcnt(3)\n" \
  "v_mfma_f32_32x32x16_bf16 v[64:79], v[12:15], v[32:35], 0\n" \
  "ds_read_b128 v[32:35], v2 offset:15360\n" \
  "v_cvt_pk_bf16_f32 v80, v80, v81\n" \
  "v_cvt_pk_bf16_f32 v81, v82, v83\n" \
  "v_cvt_pk_bf16_f32 v82, v84, v85\n" \
  "v_cvt_pk_bf16_f32 v83, v86, v87\n" \
  "v_cvt_pk_bf16_f32 v84, v88, v89\n" \
  "v_cvt_pk_bf16_f32 v85, v90, v91\n" \
  "v_cvt_pk_bf16_f32 v86, v92, v93\n" \
  "v_cvt_pk_bf16_f32 v87, v94, v95\n" \
  "v_mfma_f32_32x32x16_bf16 v[144:159], v[80:83], v[96:99], v[144:159]\n" \
  "v_xor_b32 v9, 32, v3\n" \
  "v_xor_b32 v10, 64, v3\n" \
  "v_xor_b32 v11, 96, v3\n" \
  "v_mfma_f32_32x32x16_bf16 v[144:159], v[84:87], v[100:103], v[144:159]\n" \
  "s_waitcnt lgkmcnt(3)\n" \
  "v_mfma_f32_32x32x16_bf16 v[80:95], v[16:19], v[36:39], 0\n" \
  "ds_read_b128 v[36:39], v2 offset:27648\n" \
  "v_mul_f32 v48, v48, v64\n" \
  "v_mul_f32 v49, v49, v65\n" \
  "v_mul_f32 v50, v50, v66\n" \
  "v_mul_f32 v51, v51, v67\n" \
  "v_mul_f32 v52, v52, v68\n" \
  "v_mul_f32 v53, v53, v69\n" \
  "s_waitcnt lgkmcnt(3)\n" \
  "v_mfma_f32_32x32x16_bf16 v[80:95], v[20:23], v[40:43], v[80:95]\n" \
  "v_mul_f32 v54, v54, v70\n" \
  "v_mul_f32 v55, v55, v71\n" \
  "v_mul_f32 v56, v56, v72\n" \
  "v_mul_f32 v57, v57, v73\n" \
  "v_mul_f32 v58, v58, v74\n" \
  "v_mul_f32 v59, v59, v75\n" \
  "s_waitcnt lgkmcnt(2)\n" \
  "v_mfma_f32_32x32x16_bf16 v[80:95], v[24:27], v[44:47], v[80:95]\n" \
  "v_mul_f32 v60, v60, v76\n" \
  "v_mul_f32 v61, v61, v77\n" \
  "v_mul_f32 v62, v62, v78\n" \
  "v_mul_f32 v63, v63, v79\n" \
  "s_waitcnt lgkmcnt(1)\n" \
  "v_mfma_f32_32x32x16_bf16 v[80:95], v[28:31], v[32:35], v[80:95]\n" \
  "s_waitcnt lgkmcnt(0)\n" \
  "v_mfma_f32_32x32x16_bf16 v[64:79], v[12:15], v[36:39], 0\n" \
  "s_setprio 0\n" \
  "s_waitcnt vmcnt(0)\n" \
  "ds_read_b64_tr_b16 v[104:105], v3 offset:0\n" \
  "ds_read_b64_tr_b16 v[106:107], v9 offset:256\n" \
  "ds_read_b64_tr_b16 v[108:109], v10 offset:1024\n" \
  "ds_read_b64_tr_b16 v[110:111], v11 offset:1280\n" \
  "ds_read_b64_tr_b16 v[112:113], v3 offset:2048\n" \
  "ds_read_b64_tr_b16 v[114:115], v9 offset:2304\n" \
  "ds_read_b64_tr_b16 v[116:117], v10 offset:3072\n" \
  "ds_read_b64_tr_b16 v[118:119], v11 offset:3328\n" \
  "ds_read_b64_tr_b16 v[16:17], v9 offset:0\n" \
  "ds_read_b64_tr_b16 v[18:19], v3 offset:256\n" \
  "ds_read_b64_tr_b16 v[20:21], v11 offset:1024\n" \
  "ds_read_b64_tr_b16 v[22:23], v10 offset:1280\n" \
  "ds_read_b64_tr_b16 v[24:25], v9 offset:2048\n" \
  "ds_read_b64_tr_b16 v[26:27], v3 offset:2304\n" \
  "ds_read_b64_tr_b16 v[28:29], v11 offset:3072\n" \
  "s_waitcnt lgkmcnt(14)\n" \
  "ds_read_b64_tr_b16 v[30:31], v10 offset:3328\n" \
  "v_mul_f32 v80, v80, v64\n" \
  "v_mul_f32 v81, v81, v65\n" \
  "v_mul_f32 v82, v82, v66\n" \
  "v_mul_f32 v83, v83, v67\n" \
  "v_mul_f32 v84, v84, v68\n" \
  "v_mul_f32 v85, v85, v69\n" \
  "v_mul_f32 v86, v86, v70\n" \
  "v_mul_f32 v87, v87, v71\n" \
  "v_mul_f32 v88, v88, v72\n" \
  "v_mul_f32 v89, v89, v73\n" \
  "v_mul_f32 v90, v90, v74\n" \
  "v_mul_f32 v91, v91, v75\n" \
  "v_mul_f32 v92, v92, v76\n" \
  "v_mul_f32 v93, v93, v77\n" \
  "v_mul_f32 v94, v94, v78\n" \
  "v_mul_f32 v95, v95, v79\n" \
  "s_waitcnt lgkmcnt(14)\n" \
  "ds_read_b64_tr_b16 v[32:33], v3 offset:4096\n" \
  "s_waitcnt lgkmcnt(14)\n" \
  "ds_read_b64_tr_b16 v[34:35], v9 offset:4352\n" \
  "s_waitcnt lgkmcnt(14)\n" \
  "ds_read_b64_tr_b16 v[36:37], v10 offset:5120\n" \
  "s_waitcnt lgkmcnt(14)\n" \
  "ds_read_b64_tr_b16 v[38:39], v11 offset:5376\n" \
  "s_waitcnt lgkmcnt(14)\n" \
  "ds_read_b64_tr_b16 v[40:41], v3 offset:6144\n" \
  "s_waitcnt lgkmcnt(14)\n" \
  "ds_read_b64_tr_b16 v[42:43], v9 offset:6400\n" \
  "s_waitcnt lgkmcnt(14)\n" \
  "ds_read_b64_tr_b16 v[44:45], v10 offset:7168\n" \
  "s_waitcnt lgkmcnt(14)\n" \
  "ds_read_b64_tr_b16 v[46:47], v11 offset:7424\n" \
  "v_mul_f32 v104, v104, v48\n" \
  "v_mul_f32 v105, v105, v49\n" \
  "s_waitcnt lgkmcnt(8)\n" \
  "ds_read_b64_tr_b16 v[64:65], v9 offset:4096\n" \
  "v_mul_f32 v106, v106, v50\n" \
  "ds_read_b64_tr_b16 v[66:67], v3 offset:4352\n" \
  "ds_read_b64_tr_b16 v[68:69], v11 offset:5120\n" \
  "v_mul_f32 v107, v107, v51\n" \
  "ds_read_b64_tr_b16 v[70:71], v10 offset:5376\n" \
  "ds_read_b64_tr_b16 v[72:73], v9 offset:6144\n" \
  "v_mul_f32 v108, v108, v52\n" \
  "ds_read_b64_tr_b16 v[74:75], v3 offset:6400\n" \
  "ds_read_b64_tr_b16 v[76:77], v11 offset:7168\n" \
  "v_mul_f32 v109, v109, v53\n" \
  "s_waitcnt lgkmcnt(14)\n" \
  "ds_read_b64_tr_b16 v[78:79], v10 offset:7424\n" \
  "v_mul_f32 v110, v110, v54\n" \
  "v_mul_f32 v111, v111, v55\n" \
  "v_cvt_pk_bf16_f32 v104, v104, v105\n" \
  "v_cvt_pk_bf16_f32 v105, v106, v107\n" \
  "v_cvt_pk_bf16_f32 v106, v108, v109\n" \
  "v_cvt_pk_bf16_f32 v107, v110, v111\n" \
  "v_mul_f32 v112, v112, v56\n" \
  "v_mul_f32 v113, v113, v57\n" \
  "v_mfma_f32_32x32x16_bf16 v[160:175], v[104:107], v[96:99], v[160:175]\n" \
  "v_mul_f32 v114, v114, v58\n" \
  "v_mul_f32 v115, v115, v59\n" \
  "v_mul_f32 v116, v116, v60\n" \
  "v_mul_f32 v117, v117, v61\n" \
  "v_mul_f32 v118, v118, v62\n" \
  "v_mul_f32 v119, v119, v63\n" \
  "v_cvt_pk_bf16_f32 v108, v112, v113\n" \
  "v_cvt_pk_bf16_f32 v109, v114, v115\n" \
  "v_cvt_pk_bf16_f32 v110, v116, v117\n" \
  "v_cvt_pk_bf16_f32 v111, v118, v119\n" \
  "v_mul_f32 v16, v16, v80\n" \
  "v_mul_f32 v17, v17, v81\n" \
  "v_mfma_f32_32x32x16_bf16 v[160:175], v[108:111], v[100:103], v[160:175]\n" \
  "s_waitcnt lgkmcnt(14)\n" \
  "ds_read_b64_tr_b16 v[104:105], v3 offset:8192\n" \
  "s_waitcnt lgkmcnt(14)\n" \
  "ds_read_b64_tr_b16 v[106:107], v9 offset:8448\n" \
  "v_mul_f32 v18, v18, v82\n" \
  "s_waitcnt lgkmcnt(14)\n" \
  "ds_read_b64_tr_b16 v[108:109], v10 offset:9216\n" \
  "s_waitcnt lgkmcnt(14)\n" \
  "ds_read_b64_tr_b16 v[110:111], v11 offset:9472\n" \
  "v_mul_f32 v19, v19, v83\n" \
  "s_waitcnt lgkmcnt(14)\n" \
  "ds_read_b64_tr_b16 v[112:113], v3 offset:10240\n" \
  "s_waitcnt lgkmcnt(14)\n" \
  "ds_read_b64_tr_b16 v[114:115], v9 offset:10496\n" \
  "v_mul_f32 v20, v20, v84\n" \
  "s_waitcnt lgkmcnt(14)\n" \
  "ds_read_b64_tr_b16 v[116:117], v10 offset:11264\n" \
  "s_waitcnt lgkmcnt(14)\n" \
  "ds_read_b64_tr_b16 v[118:119], v11 offset:11520\n" \
  "v_mul_f32 v21, v21, v85\n" \
  "v_mul_f32 v22, v22, v86\n" \
  "v_mul_f32 v23, v23, v87\n" \
  "v_cvt_pk_bf16_f32 v16, v16, v17\n" \
  "v_cvt_pk_bf16_f32 v17, v18, v19\n" \
  "v_cvt_pk_bf16_f32 v18, v20, v21\n" \
  "v_cvt_pk_bf16_f32 v19, v22, v23\n" \
  "v_mul_f32 v24, v24, v88\n" \
  "v_mul_f32 v25, v25, v89\n" \
  "v_mfma_f32_32x32x16_bf16 v[176:191], v[16:19], v[96:99], v[176:191]\n" \
  "v_mul_f32 v26, v26, v90\n" \
  "v_mul_f32 v27, v27, v91\n" \
  "v_mul_f32 v28, v28, v92\n" \
  "v_mul_f32 v29, v29, v93\n" \
  "v_mul_f32 v30, v30, v94\n" \
  "v_mul_f32 v31, v31, v95\n" \
  "v_cvt_pk_bf16_f32 v20, v24, v25\n" \
  "v_cvt_pk_bf16_f32 v21, v26, v27\n" \
  "v_cvt_pk_bf16_f32 v22, v28, v29\n" \
  "v_cvt_pk_bf16_f32 v23, v30, v31\n" \
  "v_mul_f32 v32, v32, v48\n" \
  "v_mul_f32 v33, v33, v49\n" \
  "v_mfma_f32_32x32x16_bf16 v[176:191], v[20:23], v[100:103], v[176:191]\n" \
  "s_waitcnt lgkmcnt(8)\n" \
  "ds_read_b64_tr_b16 v[16:17], v9 offset:8192\n" \
  "v_mul_f32 v34, v34, v50\n" \
  "ds_read_b64_tr_b16 v[18:19], v3 offset:8448\n" \
  "ds_read_b64_tr_b16 v[20:21], v11 offset:9216\n" \
  "v_mul_f32 v35, v35, v51\n" \
  "ds_read_b64_tr_b16 v[22:23], v10 offset:9472\n" \
  "ds_read_b64_tr_b16 v[24:25], v9 offset:10240\n" \
  "v_mul_f32 v36, v36, v52\n" \
  "ds_read_b64_tr_b16 v[26:27], v3 offset:10496\n" \
  "ds_read_b64_tr_b16 v[28:29], v11 offset:11264\n" \
  "v_mul_f32 v37, v37, v53\n" \
  "s_waitcnt lgkmcnt(14)\n" \
  "ds_read_b64_tr_b16 v[30:31], v10 offset:11520\n" \
  "v_mul_f32 v38, v38, v54\n" \
  "v_mul_f32 v39, v39, v55\n" \
  "v_cvt_pk_bf16_f32 v32, v32, v33\n" \
  "v_cvt_pk_bf16_f32 v33, v34, v35\n" \
  "v_cvt_pk_bf16_f32 v34, v36, v37\n" \
  "v_cvt_pk_bf16_f32 v35, v38, v39\n" \
  "v_mul_f32 v40, v40, v56\n" \
  "v_mul_f32 v41, v41, v57\n" \
  "v_mfma_f32_32x32x16_bf16 v[192:207], v[32:35], v[96:99], v[192:207]\n" \
  "v_mul_f32 v42, v42, v58\n" \
  "v_mul_f32 v43, v43, v59\n" \
  "v_mul_f32 v44, v44, v60\n" \
  "v_mul_f32 v45, v45, v61\n" \
  "v_mul_f32 v46, v46, v62\n" \
  "v_mul_f32 v47, v47, v63\n" \
  "v_cvt_pk_bf16_f32 v36, v40, v41\n" \
  "v_cvt_pk_bf16_f32 v37, v42, v43\n" \
  "v_cvt_pk_bf16_f32 v38, v44, v45\n" \
  "v_cvt_pk_bf16_f32 v39, v46, v47\n" \
  "v_mul_f32 v64, v64, v80\n" \
  "v_mul_f32 v65, v65, v81\n" \
  "v_mfma_f32_32x32x16_bf16 v[192:207], v[36:39], v[100:103], v[192:207]\n" \
  "v_mul_f32 v66, v66, v82\n" \
  "v_mul_f32 v67, v67, v83\n" \
  "v_mul_f32 v68, v68, v84\n" \
  "v_mul_f32 v69, v69, v85\n" \
  "v_mul_f32 v70, v70, v86\n" \
  "v_mul_f32 v71, v71, v87\n" \
  "v_cvt_pk_bf16_f32 v64, v64, v65\n" \
  "v_cvt_pk_bf16_f32 v65, v66, v67\n" \
  "v_cvt_pk_bf16_f32 v66, v68, v69\n" \
  "v_cvt_pk_bf16_f32 v67, v70, v71\n" \
  "v_mul_f32 v72, v72, v88\n" \
  "v_mul_f32 v73, v73, v89\n" \
  "v_mfma_f32_32x32x16_bf16 v[208:223], v[64:67], v[96:99], v[208:223]\n" \
  "v_mul_f32 v74, v74, v90\n" \
  "v_mul_f32 v75, v75, v91\n" \
  "v_mul_f32 v76, v76, v92\n" \
  "v_mul_f32 v77, v77, v93\n" \
  "v_mul_f32 v78, v78, v94\n" \
  "v_mul_f32 v79, v79, v95\n" \
  "v_cvt_pk_bf16_f32 v68, v72, v73\n" \
  "v_cvt_pk_bf16_f32 v69, v74, v75\n" \
  "v_cvt_pk_bf16_f32 v70, v76, v77\n" \
  "v_cvt_pk_bf16_f32 v71, v78, v79\n" \
  "v_mul_f32 v104, v104, v48\n" \
  "v_mul_f32 v105, v105, v49\n" \
  "v_mfma_f32_32x32x16_bf16 v[208:223], v[68:71], v[100:103], v[208:223]\n" \
  "s_waitcnt lgkmcnt(0)\n" \
  "v_mul_f32 v106, v106, v50\n" \
  "v_mul_f32 v107, v107, v51\n" \
  "v_mul_f32 v108, v108, v52\n" \
  "v_mul_f32 v109, v109, v53\n" \
  "v_mul_f32 v110, v110, v54\n" \
  "v_mul_f32 v111, v111, v55\n" \
  "v_cvt_pk_bf16_f32 v104, v104, v105\n" \
  "v_cvt_pk_bf16_f32 v105, v106, v107\n" \
  "v_cvt_pk_bf16_f32 v106, v108, v109\n" \
  "v_cvt_pk_bf16_f32 v107, v110, v111\n" \
  "v_mul_f32 v112, v112, v56\n" \
  "v_mul_f32 v113, v113, v57\n" \
  "v_mfma_f32_32x32x16_bf16 v[224:239], v[104:107], v[96:99], v[224:239]\n" \
  "v_mul_f32 v114, v114, v58\n" \
  "v_mul_f32 v115, v115, v59\n" \
  "v_mul_f32 v116, v116, v60\n" \
  "v_mul_f32 v117, v117, v61\n" \
  "v_mul_f32 v118, v118, v62\n" \
  "v_mul_f32 v119, v119, v63\n" \
  "v_cvt_pk_bf16_f32 v108, v112, v113\n" \
  "v_cvt_pk_bf16_f32 v109, v114, v115\n" \
  "v_cvt_pk_bf16_f32 v110, v116, v117\n" \
  "v_cvt_pk_bf16_f32 v111, v118, v119\n" \
  "v_mul_f32 v16, v16, v80\n" \
  "v_mul_f32 v17, v17, v81\n" \
  "v_mfma_f32_32x32x16_bf16 v[224:239], v[108:111], v[100:103], v[224:239]\n" \
  "v_mul_f32 v18, v18, v82\n" \
  "v_mul_f32 v19, v19, v83\n" \
  "v_mul_f32 v20, v20, v84\n" \
  "v_mul_f32 v21, v21, v85\n" \
  "v_mul_f32 v22, v22, v86\n" \
  "v_mul_f32 v23, v23, v87\n" \
  "v_cvt_pk_bf16_f32 v16, v16, v17\n" \
  "v_cvt_pk_bf16_f32 v17, v18, v19\n" \
  "v_cvt_pk_bf16_f32 v18, v20, v21\n" \
  "v_cvt_pk_bf16_f32 v19, v22, v23\n" \
  "v_mul_f32 v24, v24, v88\n" \
  "v_mul_f32 v25, v25, v89\n" \
  "v_mfma_f32_32x32x16_bf16 v[240:255], v[16:19], v[96:99], v[240:255]\n" \
  "v_mul_f32 v26, v26, v90\n" \
  "v_mul_f32 v27, v27, v91\n" \
  "v_mul_f32 v28, v28, v92\n" \
  "v_mul_f32 v29, v29, v93\n" \
  "v_mul_f32 v30, v30, v94\n" \
  "v_mul_f32 v31, v31, v95\n" \
  "v_cvt_pk_bf16_f32 v20, v24, v25\n" \
  "v_cvt_pk_bf16_f32 v21, v26, v27\n" \
  "v_cvt_pk_bf16_f32 v22, v28, v29\n" \
  "v_cvt_pk_bf16_f32 v23, v30, v31\n" \
  "s_nop 1\n" \
  "v_mfma_f32_32x32x16_bf16 v[240:255], v[20:23], v[100:103], v[240:255]\n" \
  "s_branch .Lend%=\n" \
  ".Lhalf%=:\n" \
  "s_setprio 1\n" \
  "ds_read_b128 v[80:83], v5 offset:0\n" \
  "v_xor_b32 v9, 16, v1\n" \
  "v_xor_b32 v10, 32, v1\n" \
  "v_xor_b32 v11, 48, v1\n" \
  "s_waitcnt vmcnt(12)\n" \
  "ds_read_b128 v[16:19], v1 offset:0\n" \
  "ds_read_b128 v[32:35], v2 offset:16384\n" \
  "ds_read_b128 v[20:23], v9 offset:0\n" \
  "ds_read_b128 v[36:39], v2 offset:17408\n" \
  "ds_read_b128 v[24:27], v10 offset:0\n" \
  "ds_read_b128 v[40:43], v2 offset:18432\n" \
  "ds_read_b128 v[28:31], v11 offset:0\n" \
  "ds_read_b128 v[44:47], v2 offset:19456\n" \
  "ds_read_b128 v[120:123], v5 offset:128\n" \
  "s_waitcnt lgkmcnt(9)\n" \
  "v_xor_b32 v96, v80, v4\n" \
  "v_pk_sub_u16 v96, 1, v96 op_sel_hi:[0,1] clamp\n" \
  "v_xor_b32 v97, v81, v4\n" \
  "v_pk_sub_u16 v97, 1, v97 op_sel_hi:[0,1] clamp\n" \
  "v_xor_b32 v98, v82, v4\n" \
  "v_pk_sub_u16 v98, 1, v98 op_sel_hi:[0,1] clamp\n" \
  "v_xor_b32 v99, v83, v4\n" \
  "v_pk_sub_u16 v99, 1, v99 op_sel_hi:[0,1] clamp\n" \
  "s_waitcnt lgkmcnt(7)\n" \
  "v_mfma_f32_32x32x16_bf16 v[48:63], v[16:19], v[32:35], 0\n" \
  "s_cbranch_vccz .Lfh2%=\n" \
  "s_add_u32 m0, %[buf], 7936\n" \
  "s_nop 0\n" \
  "buffer_load_dwordx4 v112, %[rg], 0 offen offset:256 lds\n" \
  "buffer_load_dwordx4 v113, %[rg], 0 offen offset:1280 lds\n" \
  "buffer_load_dwordx4 v114, %[rg], 0 offen offset:2304 lds\n" \
  "buffer_load_dwordx4 v115, %[rg], 0 offen offset:3328 lds\n" \
  ".Lfh2%=:\n" \
  "ds_read_b128 v[32:35], v2 offset:28672\n" \
  "s_waitcnt lgkmcnt(1)\n" \
  "v_pk_mul_lo_u16 v120, v96, v120\n" \
  "v_pk_mul_lo_u16 v121, v97, v121\n" \
  "v_pk_mul_lo_u16 v122, v98, v122\n" \
  "v_pk_mul_lo_u16 v123, v99, v123\n" \
  "v_mfma_f32_32x32x16_bf16 v[48:63], v[20:23], v[36:39], v[48:63]\n" \
  "s_cbranch_vccz .Lfh3%=\n" \
  "s_add_u32 m0, %[buf], 11904\n" \
  "s_nop 0\n" \
  "buffer_load_dwordx4 v112, %[rg], 0 offen offset:384 lds\n" \
  "buffer_load_dwordx4 v113, %[rg], 0 offen offset:1408 lds\n" \
  "buffer_load_dwordx4 v114, %[rg], 0 offen offset:2432 lds\n" \
  "buffer_load_dwordx4 v115, %[rg], 0 offen offset:3456 lds\n" \
  ".Lfh3%=:\n" \
  "ds_read_b128 v[36:39], v2 offset:20480\n" \
  "v_mfma_f32_32x32x16_bf16 v[48:63], v[24:27], v[40:43], v[48:63]\n" \
  "ds_read_b128 v[40:43], v2 offset:21504\n" \
  "v_mfma_f32_32x32x16_bf16 v[48:63], v[28:31], v[44:47], v[48:63]\n" \
  "ds_read_b128 v[44:47], v2 offset:22528\n" \
  "s_waitcnt lgkmcnt(3)\n" \
  "v_mfma_f32_32x32x16_bf16 v[64:79], v[12:15], v[32:35], 0\n" \
  "ds_read_b128 v[32:35], v2 offset:23552\n" \
  "s_mov_b64 vcc, 0\n" \
  "s_waitcnt lgkmcnt(3)\n" \
  "v_mfma_f32_32x32x16_bf16 v[80:95], v[16:19], v[36:39], 0\n" \
  "ds_read_b128 v[36:39], v2 offset:29696\n" \
  "s_waitcnt lgkmcnt(3)\n" \
  "v_mfma_f32_32x32x16_bf16 v[80:95], v[20:23], v[40:43], v[80:95]\n" \
  "ds_read_b128 v[40:43], v2 offset:0\n" \
  "s_nop 3\n" \
  "v_mul_f32 v48, v48, v64\n" \
  "v_mul_f32 v49, v49, v65\n" \
  "v_mul_f32 v50, v50, v66\n" \
  "v_mul_f32 v51, v51, v67\n" \
  "v_mul_f32 v52, v52, v68\n" \
  "v_mul_f32 v53, v53, v69\n" \
  "s_waitcnt lgkmcnt(3)\n" \
  "v_mfma_f32_32x32x16_bf16 v[80:95], v[24:27], v[44:47], v[80:95]\n" \
  "ds_read_b128 v[44:47], v2 offset:1024\n" \
  "v_mul_f32 v54, v54, v70\n" \
  "v_mul_f32 v55, v55, v71\n" \
  "s_waitcnt lgkmcnt(3)\n" \
  "v_mfma_f32_32x32x16_bf16 v[80:95], v[28:31], v[32:35], v[80:95]\n" \
  "ds_read_b128 v[32:35], v2 offset:2048\n" \
  "s_waitcnt lgkmcnt(3)\n" \
  "v_mfma_f32_32x32x16_bf16 v[64:79], v[12:15], v[36:39], 0\n" \
  "ds_read_b128 v[36:39], v2 offset:3072\n" \
  "v_cvt_pk_bf16_f32 v104, v48, v49\n" \
  "v_cvt_pk_bf16_f32 v105, v50, v51\n" \
  "v_cvt_pk_bf16_f32 v106, v52, v53\n" \
  "v_cvt_pk_bf16_f32 v107, v54, v55\n" \
  "s_waitcnt lgkmcnt(3)\n" \
  "v_mfma_f32_32x32x16_bf16 v[48:63], v[16:19], v[40:43], 0\n" \
  "ds_read_b128 v[40:43], v2 offset:24576\n" \
  "s_waitcnt lgkmcnt(3)\n" \
  "v_mfma_f32_32x32x16_bf16 v[48:63], v[20:23], v[44:47], v[48:63]\n" \
  "ds_read_b128 v[44:47], v2 offset:4096\n" \
  "s_nop 0\n" \
  "v_mul_f32 v80, v80, v64\n" \
  "v_mul_f32 v81, v81, v65\n" \
  "v_mul_f32 v82, v82, v66\n" \
  "v_mul_f32 v83, v83, v67\n" \
  "v_mul_f32 v84, v84, v68\n" \
  "v_mul_f32 v85, v85, v69\n" \
  "s_waitcnt lgkmcnt(3)\n" \
  "v_mfma_f32_32x32x16_bf16 v[48:63], v[24:27], v[32:35], v[48:63]\n" \
  "ds_read_b128 v[32:35], v2 offset:5120\n" \
  "v_mul_f32 v86, v86, v70\n" \
  "v_mul_f32 v87, v87, v71\n" \
  "s_waitcnt lgkmcnt(3)\n" \
  "v_mfma_f32_32x32x16_bf16 v[48:63], v[28:31], v[36:39], v[48:63]\n" \
  "ds_read_b128 v[36:39], v2 offset:6144\n" \
  "s_waitcnt lgkmcnt(3)\n" \
  "v_mfma_f32_32x32x16_bf16 v[64:79], v[12:15], v[40:43], 0\n" \
  "ds_read_b128 v[40:43], v2 offset:7168\n" \
  "v_cvt_pk_bf16_f32 v112, v80, v81\n" \
  "v_cvt_pk_bf16_f32 v113, v82, v83\n" \
  "v_cvt_pk_bf16_f32 v114, v84, v85\n" \
  "v_cvt_pk_bf16_f32 v115, v86, v87\n" \
  "ds_read_b128 v[80:83], v5 offset:192\n" \
  "ds_read_b128 v[88:91], v5 offset:256\n" \
  "v_mfma_f32_32x32x16_bf16 v[160:175], v[104:107], v[120:123], v[160:175]\n" \
  "v_mfma_f32_32x32x16_bf16 v[176:191], v[112:115], v[120:123], v[176:191]\n" \
  "s_waitcnt lgkmcnt(1)\n" \
  "v_pk_mul_lo_u16 v120, v96, v80\n" \
  "v_pk_mul_lo_u16 v121, v97, v81\n" \
  "v_pk_mul_lo_u16 v122, v98, v82\n" \
  "v_pk_mul_lo_u16 v123, v99, v83\n" \
  "s_nop 1\n" \
  "v_mfma_f32_32x32x16_bf16 v[192:207], v[104:107], v[120:123], v[192:207]\n" \
  "v_mul_f32 v48, v48, v64\n" \
  "v_mul_f32 v49, v49, v65\n" \
  "v_mul_f32 v50, v50, v66\n" \
  "v_mul_f32 v51, v51, v67\n" \
  "v_mul_f32 v52, v52, v68\n" \
  "v_mul_f32 v53, v53, v69\n" \
  "v_mfma_f32_32x32x16_bf16 v[208:223], v[112:115], v[120:123], v[208:223]\n" \
  "s_waitcnt lgkmcnt(0)\n" \
  "v_pk_mul_lo_u16 v120, v96, v88\n" \
  "v_pk_mul_lo_u16 v121, v97, v89\n" \
  "v_pk_mul_lo_u16 v122, v98, v90\n" \
  "v_pk_mul_lo_u16 v123, v99, v91\n" \
  "v_mul_f32 v54, v54, v70\n" \
  "v_mul_f32 v55, v55, v71\n" \
  "v_mfma_f32_32x32x16_bf16 v[224:239], v[104:107], v[120:123], v[224:239]\n" \
  "v_cvt_pk_bf16_f32 v48, v48, v49\n" \
  "v_cvt_pk_bf16_f32 v49, v50, v51\n" \
  "v_cvt_pk_bf16_f32 v50, v52, v53\n" \
  "v_cvt_pk_bf16_f32 v51, v54, v55\n" \
  "v_mfma_f32_32x32x16_bf16 v[240:255], v[112:115], v[120:123], v[240:255]\n" \
  "ds_read_b128 v[120:123], v5 offset:64\n" \
  "v_mfma_f32_32x32x16_bf16 v[80:95], v[16:19], v[44:47], 0\n" \
  "ds_read_b128 v[44:47], v2 offset:25600\n" \
  "v_mfma_f32_32x32x16_bf16 v[80:95], v[20:23], v[32:35], v[80:95]\n" \
  "ds_read_b128 v[32:35], v2 offset:8192\n" \
  "v_mfma_f32_32x32x16_bf16 v[80:95], v[24:27], v[36:39], v[80:95]\n" \
  "ds_read_b128 v[36:39], v2 offset:9216\n" \
  "s_waitcnt lgkmcnt(3)\n" \
  "v_pk_mul_lo_u16 v96, v96, v120\n" \
  "v_pk_mul_lo_u16 v97, v97, v121\n" \
  "v_pk_mul_lo_u16 v98, v98, v122\n" \
  "v_pk_mul_lo_u16 v99, v99, v123\n" \
  "v_mfma_f32_32x32x16_bf16 v[80:95], v[28:31], v[40:43], v[80:95]\n" \
  "ds_read_b128 v[40:43], v2 offset:10240\n" \
  "s_waitcnt lgkmcnt(3)\n" \
  "v_mfma_f32_32x32x16_bf16 v[64:79], v[12:15], v[44:47], 0\n" \
  "ds_read_b128 v[44:47], v2 offset:11264\n" \
  "v_mfma_f32_32x32x16_bf16 v[128:143], v[48:51], v[96:99], v[128:143]\n" \
  "s_waitcnt lgkmcnt(3)\n" \
  "v_mfma_f32_32x32x16_bf16 v[48:63], v[16:19], v[32:35], 0\n" \
  "ds_read_b128 v[32:35], v2 offset:26624\n" \
  "s_nop 6\n" \
  "v_mul_f32 v80, v80, v64\n" \
  "v_mul_f32 v81, v81, v65\n" \
  "v_mul_f32 v82, v82, v66\n" \
  "v_mul_f32 v83, v83, v67\n" \
  "v_mul_f32 v84, v84, v68\n" \
  "v_mul_f32 v85, v85, v69\n" \
  "s_waitcnt lgkmcnt(3)\n" \
  "v_mfma_f32_32x32x16_bf16 v[48:63], v[20:23], v[36:39], v[48:63]\n" \
  "ds_read_b128 v[36:39], v2 offset:12288\n" \
  "v_mul_f32 v86, v86, v70\n" \
  "v_mul_f32 v87, v87, v71\n" \
  "s_waitcnt lgkmcnt(3)\n" \
  "v_mfma_f32_32x32x16_bf16 v[48:63], v[24:27], v[40:43], v[48:63]\n" \
  "ds_read_b128 v[40:43], v2 offset:13312\n" \
  "s_waitcnt lgkmcnt(3)\n" \
  "v_mfma_f32_32x32x16_bf16 v[48:63], v[28:31], v[44:47], v[48:63]\n" \
  "ds_read_b128 v[44:47], v2 offset:14336\n" \
  "s_waitcnt lgkmcnt(3)\n" \
  "v_mfma_f32_32x32x16_bf16 v[64:79], v[12:15], v[32:35], 0\n" \
  "ds_read_b128 v[32:35], v2 offset:15360\n" \
  "v_cvt_pk_bf16_f32 v80, v80, v81\n" \
  "v_cvt_pk_bf16_f32 v81, v82, v83\n" \
  "v_cvt_pk_bf16_f32 v82, v84, v85\n" \
  "v_cvt_pk_bf16_f32 v83, v86, v87\n" \
  "s_nop 1\n" \
  "v_mfma_f32_32x32x16_bf16 v[144:159], v[80:83], v[96:99], v[144:159]\n" \
  "v_xor_b32 v9, 32, v3\n" \
  "v_xor_b32 v10, 64, v3\n" \
  "v_xor_b32 v11, 96, v3\n" \
  "s_waitcnt lgkmcnt(3)\n" \
  "v_mfma_f32_32x32x16_bf16 v[80:95], v[16:19], v[36:39], 0\n" \
  "ds_read_b128 v[36:39], v2 offset:27648\n" \
  "v_mul_f32 v48, v48, v64\n" \
  "v_mul_f32 v49, v49, v65\n" \
  "v_mul_f32 v50, v50, v66\n" \
  "v_mul_f32 v51, v51, v67\n" \
  "v_mul_f32 v52, v52, v68\n" \
  "v_mul_f32 v53, v53, v69\n" \
  "s_waitcnt lgkmcnt(3)\n" \
  "v_mfma_f32_32x32x16_bf16 v[80:95], v[20:23], v[40:43], v[80:95]\n" \
  "v_mul_f32 v54, v54, v70\n" \
  "v_mul_f32 v55, v55, v71\n" \
  "s_waitcnt lgkmcnt(2)\n" \
  "v_mfma_f32_32x32x16_bf16 v[80:95], v[24:27], v[44:47], v[80:95]\n" \
  "s_waitcnt lgkmcnt(1)\n" \
  "v_mfma_f32_32x32x16_bf16 v[80:95], v[28:31], v[32:35], v[80:95]\n" \
  "s_waitcnt lgkmcnt(0)\n" \
  "v_mfma_f32_32x32x16_bf16 v[64:79], v[12:15], v[36:39], 0\n" \
  "s_setprio 0\n" \
  "s_waitcnt vmcnt(0)\n" \
  "ds_read_b64_tr_b16 v[104:105], v3 offset:0\n" \
  "ds_read_b64_tr_b16 v[106:107], v9 offset:256\n" \
  "ds_read_b64_tr_b16 v[108:109], v10 offset:1024\n" \
  "ds_read_b64_tr_b16 v[110:111], v11 offset:1280\n" \
  "ds_read_b64_tr_b16 v[16:17], v9 offset:0\n" \
  "ds_read_b64_tr_b16 v[18:19], v3 offset:256\n" \
  "ds_read_b64_tr_b16 v[20:21], v11 offset:1024\n" \
  "ds_read_b64_tr_b16 v[22:23], v10 offset:1280\n" \
  "s_nop 1\n" \
  "v_mul_f32 v80, v80, v64\n" \
  "v_mul_f32 v81, v81, v65\n" \
  "v_mul_f32 v82, v82, v66\n" \
  "v_mul_f32 v83, v83, v67\n" \
  "v_mul_f32 v84, v84, v68\n" \
  "v_mul_f32 v85, v85, v69\n" \
  "v_mul_f32 v86, v86, v70\n" \
  "v_mul_f32 v87, v87, v71\n" \
  "ds_read_b64_tr_b16 v[32:33], v3 offset:4096\n" \
  "ds_read_b64_tr_b16 v[34:35], v9 offset:4352\n" \
  "ds_read_b64_tr_b16 v[36:37], v10 offset:5120\n" \
  "ds_read_b64_tr_b16 v[38:39], v11 offset:5376\n" \
  "s_waitcnt lgkmcnt(11)\n" \
  "v_mul_f32 v104, v104, v48\n" \
  "v_mul_f32 v105, v105, v49\n" \
  "s_waitcnt lgkmcnt(4)\n" \
  "ds_read_b64_tr_b16 v[64:65], v9 offset:4096\n" \
  "v_mul_f32 v106, v106, v50\n" \
  "ds_read_b64_tr_b16 v[66:67], v3 offset:4352\n" \
  "ds_read_b64_tr_b16 v[68:69], v11 offset:5120\n" \
  "v_mul_f32 v107, v107, v51\n" \
  "ds_read_b64_tr_b16 v[70:71], v10 offset:5376\n" \
  "v_mul_f32 v108, v108, v52\n" \
  "v_mul_f32 v109, v109, v53\n" \
  "v_mul_f32 v110, v110, v54\n" \
  "v_mul_f32 v111, v111, v55\n" \
  "v_cvt_pk_bf16_f32 v104, v104, v105\n" \
  "v_cvt_pk_bf16_f32 v105, v106, v107\n" \
  "v_cvt_pk_bf16_f32 v106, v108, v109\n" \
  "v_cvt_pk_bf16_f32 v107, v110, v111\n" \
  "v_mul_f32 v16, v16, v80\n" \
  "v_mul_f32 v17, v17, v81\n" \
  "v_mfma_f32_32x32x16_bf16 v[160:175], v[104:107], v[96:99], v[160:175]\n" \
  "ds_read_b64_tr_b16 v[104:105], v3 offset:8192\n" \
  "ds_read_b64_tr_b16 v[106:107], v9 offset:8448\n" \
  "v_mul_f32 v18, v18, v82\n" \
  "ds_read_b64_tr_b16 v[108:109], v10 offset:9216\n" \
  "ds_read_b64_tr_b16 v[110:111], v11 offset:9472\n" \
  "v_mul_f32 v19, v19, v83\n" \
  "v_mul_f32 v20, v20, v84\n" \
  "v_mul_f32 v21, v21, v85\n" \
  "v_mul_f32 v22, v22, v86\n" \
  "v_mul_f32 v23, v23, v87\n" \
  "v_cvt_pk_bf16_f32 v16, v16, v17\n" \
  "v_cvt_pk_bf16_f32 v17, v18, v19\n" \
  "v_cvt_pk_bf16_f32 v18, v20, v21\n" \
  "v_cvt_pk_bf16_f32 v19, v22, v23\n" \
  "s_waitcnt lgkmcnt(11)\n" \
  "v_mul_f32 v32, v32, v48\n" \
  "v_mul_f32 v33, v33, v49\n" \
  "v_mfma_f32_32x32x16_bf16 v[176:191], v[16:19], v[96:99], v[176:191]\n" \
  "s_waitcnt lgkmcnt(4)\n" \
  "ds_read_b64_tr_b16 v[16:17], v9 offset:8192\n" \
  "v_mul_f32 v34, v34, v50\n" \
  "ds_read_b64_tr_b16 v[18:19], v3 offset:8448\n" \
  "ds_read_b64_tr_b16 v[20:21], v11 offset:9216\n" \
  "v_mul_f32 v35, v35, v51\n" \
  "ds_read_b64_tr_b16 v[22:23], v10 offset:9472\n" \
  "v_mul_f32 v36, v36, v52\n" \
  "v_mul_f32 v37, v37, v53\n" \
  "v_mul_f32 v38, v38, v54\n" \
  "v_mul_f32 v39, v39, v55\n" \
  "v_cvt_pk_bf16_f32 v32, v32, v33\n" \
  "v_cvt_pk_bf16_f32 v33, v34, v35\n" \
  "v_cvt_pk_bf16_f32 v34, v36, v37\n" \
  "v_cvt_pk_bf16_f32 v35, v38, v39\n" \
  "v_mul_f32 v64, v64, v80\n" \
  "v_mul_f32 v65, v65, v81\n" \
  "v_mfma_f32_32x32x16_bf16 v[192:207], v[32:35], v[96:99], v[192:207]\n" \
  "v_mul_f32 v66, v66, v82\n" \
  "v_mul_f32 v67, v67, v83\n" \
  "v_mul_f32 v68, v68, v84\n" \
  "v_mul_f32 v69, v69, v85\n" \
  "v_mul_f32 v70, v70, v86\n" \
  "v_mul_f32 v71, v71, v87\n" \
  "v_cvt_pk_bf16_f32 v64, v64, v65\n" \
  "v_cvt_pk_bf16_f32 v65, v66, v67\n" \
  "v_cvt_pk_bf16_f32 v66, v68, v69\n" \
  "v_cvt_pk_bf16_f32 v67, v70, v71\n" \
  "s_waitcnt lgkmcnt(7)\n" \
  "v_mul_f32 v104, v104, v48\n" \
  "v_mul_f32 v105, v105, v49\n" \
  "v_mfma_f32_32x32x16_bf16 v[208:223], v[64:67], v[96:99], v[208:223]\n" \
  "s_waitcnt lgkmcnt(0)\n" \
  "v_mul_f32 v106, v106, v50\n" \
  "v_mul_f32 v107, v107, v51\n" \
  "v_mul_f32 v108, v108, v52\n" \
  "v_mul_f32 v109, v109, v53\n" \
  "v_mul_f32 v110, v110, v54\n" \
  "v_mul_f32 v111, v111, v55\n" \
  "v_cvt_pk_bf16_f32 v104, v104, v105\n" \
  "v_cvt_pk_bf16_f32 v105, v106, v107\n" \
  "v_cvt_pk_bf16_f32 v106, v108, v109\n" \
  "v_cvt_pk_bf16_f32 v107, v110, v111\n" \
  "v_mul_f32 v16, v16, v80\n" \
  "v_mul_f32 v17, v17, v81\n" \
  "v_mfma_f32_32x32x16_bf16 v[224:239], v[104:107], v[96:99], v[224:239]\n" \
  "v_mul_f32 v18, v18, v82\n" \
  "v_mul_f32 v19, v19, v83\n" \
  "v_mul_f32 v20, v20, v84\n" \
  "v_mul_f32 v21, v21, v85\n" \
  "v_mul_f32 v22, v22, v86\n" \
  "v_mul_f32 v23, v23, v87\n" \
  "v_cvt_pk_bf16_f32 v16, v16, v17\n" \
  "v_cvt_pk_bf16_f32 v17, v18, v19\n" \
  "v_cvt_pk_bf16_f32 v18, v20, v21\n" \
  "v_cvt_pk_bf16_f32 v19, v22, v23\n" \
  "s_nop 1\n" \
  "v_mfma_f32_32x32x16_bf16 v[240:255], v[16:19], v[96:99], v[240:255]\n" \
  ".Lend%=:\n" \
  "s_nop 11\n" \
  ""

#define CL8(a) "v" #a "0", "v" #a "1", "v" #a "2", "v" #a "3", "v" #a "4", "v" #a "5", "v" #a "6", "v" #a "7"
#define CL10(a) CL8(a), "v" #a "8", "v" #a "9"

__global__ __launch_bounds__(K2T, 2) void node_update(const char* __restrict__ sv, const char* __restrict__ pos,
                                                      const char* __restrict__ ws, char* __restrict__ out,
                                                      int N, int P) {
  extern __shared__ __attribute__((aligned(128))) char smem[];
  const unsigned lb = (unsigned)(size_t)(LAS char*)smem;
  const int tid = threadIdx.x, l = tid & 63, w = __builtin_amdgcn_readfirstlane(tid >> 6);
  const int bin = blockIdx.x, e = l & 31, h = l >> 5;
  const unsigned lst = lb + L_LIST + w * 1024;
  const char* cellp = ws + WS_CELLA + (size_t)bin * (MAXP * 32) + min(w * 52 + min(l, 51), 2 * MAXP - 1) * 16;
  u32x4 ca;
  u32x4 cb;
  if (w < NWIN) asm volatile("global_load_dwordx4 %0, %2, off\n\tglobal_load_dwordx4 %1, %3, off"
                             : "=&v"(ca), "=&v"(cb) : "v"(cellp), "v"(cellp + (WS_CELLB - WS_CELLA)) : "memory");
  if (l < 8) sts1(lb + L_CTR + l * 32 + w * 4, 0u);
  wg_barrier();
  if (w == NWIN) {
    int l;
    asm volatile("v_mbcnt_lo_u32_b32 %0, -1, 0\n\tv_mbcnt_hi_u32_b32 %0, -1, %0" : "=v"(l));
    const __amdgpu_buffer_rsrc_t rsv = biased_rsrc(sv, N * 512);
    const __amdgpu_buffer_rsrc_t rws = __builtin_amdgcn_make_buffer_rsrc((void*)ws, (short)0, NFRAG * 1024, 0x00020000);
    const unsigned c0 = (unsigned)(((l & 7) ^ (((l >> 4) & 1) << 1) ^ (l >> 5)) * 16);
    auto offs = [&](int i) {
      const unsigned r0 = (unsigned)(bin * NPB + i * NPW + (l >> 3)) * 512u;
      return row_offsets(r0, r0 + 4096, r0 + 8192, l < 32 ? r0 + 12288 : 0xE0000000u, c0 | 0xC00u);
    };
#pragma unroll 1
    for (int i = 0; i < NWIN; ++i) dma_part<0, 0>(1, lb + L_BUF + i * 16384, offs(i), rsv);
#pragma unroll 1
    for (int i = NWIN - 1; i >= 0; --i) {
      const unsigned bf = lb + L_BUF + i * 16384;
      const u32x4 vo = offs(i);
      dma_part<1, 0>(1, bf, vo, rsv); dma_part<2, 0>(1, bf, vo, rsv); dma_part<3, 0>(1, bf, vo, rsv);
      if (i == NWIN - LBAR) asm volatile("s_waitcnt vmcnt(%0)\n\ts_barrier" :: "i"(12 * LBAR) : "memory");
    }
#pragma unroll 1
    for (int f = 0; f < 28; f += 4) dma_lin4(lb + L_W + f * 1024, l * 16, f * 1024, rws);
    dma_lin(lb + L_W + 28 * 1024, l * 16, 28 * 1024, rws); dma_lin(lb + L_W + 29 * 1024, l * 16, 29 * 1024, rws);
    asm volatile("s_waitcnt vmcnt(%0)\n\ts_barrier\n\ts_waitcnt vmcnt(0)\n\ts_barrier\npainn_code_end2:" :: "i"(NFRAG) : "memory");
    return;
  }
  {
    const u32x4 sent = {SENT, SENT, SENT, SENT};
    sts4(lst + l * 16, sent);
  }
  bf16x8 id[2];
#pragma unroll
  for (int s2 = 0; s2 < 2; ++s2) {
    const int q = e - 16 * s2 - 4 * h;
    u32x4 x;
    x.x = q == 0 ? 0x3f80u : q == 1 ? 0x3f800000u : 0u; x.y = q == 2 ? 0x3f80u : q == 3 ? 0x3f800000u : 0u;
    x.z = q == 8 ? 0x3f80u : q == 9 ? 0x3f800000u : 0u; x.w = q == 10 ? 0x3f80u : q == 11 ? 0x3f800000u : 0u;
    id[s2] = __builtin_bit_cast(bf16x8, x);
  }
  PIN("+v"(id[0]), "+v"(id[1]));
  const unsigned cslot = w * 26 + (l >> 1), cq = cslot / PSL;
  const bool hascell = l < 52 && cslot < MAXP && cq + 8 * (cslot - PSL * cq) < (unsigned)P;
  asm volatile("s_waitcnt vmcnt(0)" : "+v"(ca), "+v"(cb) :: "memory");
  if (!hascell) ca = (u32x4){SENT, SENT, SENT, SENT};
  const unsigned mark = __builtin_amdgcn_update_dpp(0u, ca.w, 0xF5, 0xf, 0xf, true);
  const bool ismark = mark >= 0xE0000000u && mark != SENT;
  auto insert = [&](u32x4 c) {
    unsigned ps[4], wo[4];
#pragma unroll
    for (int k = 0; k < 4; ++k) {
      wo[k] = c[k] < 0xE0000000u ? (c[k] >> 29) * 32 : 0x40000u;
      ps[k] = lds_inc(lb + L_CTR + wo[k] + w * 4);
      __hip_atomic_fetch_add((LAS unsigned*)(size_t)(lb + L_CTR + wo[k] + 28), 1u, __ATOMIC_RELAXED, __HIP_MEMORY_SCOPE_WORKGROUP);
    }
#pragma unroll
    for (int k = 0; k < 4; ++k) sts1(lb + L_SUB + wo[k] * 48 + w * 128 + min(ps[k], 0x4000u) * (ps[k] < 32 ? 4 : 0x10000u), c[k]);
  };
  insert(ca);
  if (__any(ismark)) {
    if (!ismark) cb = (u32x4){SENT, SENT, SENT, SENT};
    insert(cb);
    if (__any(ismark && (mark & 0x1fffu) > 15) && l < NWIN) sts1(lb + L_CTR + l * 32 + w * 4, 0x10000u);
  }
  wg_barrier();
  int win;
  {
    const unsigned i = l >> 3, j = l & 7;
    const unsigned ti = lds1(lb + L_CTR + i * 32 + 28) * 8 + (7 - i), tj = lds1(lb + L_CTR + j * 32 + 28) * 8 + (7 - j);
    const unsigned long long gt = __ballot(j < NWIN && tj > ti);
    const unsigned rank = __builtin_popcount((unsigned)(gt >> (8 * j)) & 0xffu);
    const unsigned want = w == 3 ? 0u : w < 3 ? w + 1 : 10 - w;
    win = __builtin_amdgcn_readfirstlane(__builtin_ctzll(__ballot(l < NWIN && rank == want)));
  }
  const int node0 = bin * NPB + win * NPW;
  const unsigned buf = __builtin_amdgcn_readfirstlane(lb + L_BUF + win * 16384);
  const unsigned scr = lb + L_SUB + win * 1536;
  unsigned nrec;
  unsigned grp;
  {
    unsigned cj = lds1(lb + L_CTR + win * 32 + (l >> 3) * 4);
    const u32x4 sb = lds4(scr + l * 16);
    if (l >= 56) cj = 0;
    const unsigned inc = dpp_scan_add((l & 7) == 0 ? cj : 0u);
    const bool more = __ballot(cj > 32) != 0ull;
    grp = __builtin_amdgcn_readfirstlane(more ? 0u : 4u);
    const unsigned i0 = (l & 7) * 4, o = lst + (inc - cj + i0) * 4;
#pragma unroll
    for (int k = 0; k < 4; ++k) sts1(!more && i0 + k < cj ? o + k * 4 : 0x40000u, sb[k]);
    nrec = __builtin_amdgcn_readfirstlane(more ? 0u : __builtin_amdgcn_readlane(inc, 63));
  }
  wave_sync();
  const __amdgpu_buffer_rsrc_t rh = biased_rsrc(ws + WS_H, N * 128);
  const __amdgpu_buffer_rsrc_t rg = biased_rsrc(sv, N * 512);
  const __amdgpu_buffer_rsrc_t rpos = __builtin_amdgcn_make_buffer_rsrc((void*)pos, (short)0, (N * 6 + 3) & ~3, 0x00020000);
  const __amdgpu_buffer_rsrc_t rtab = __builtin_amdgcn_make_buffer_rsrc((void*)(ws + WS_TAB), (short)0, NTAB * 32, 0x00020000);

  u32x2 raw[32];
  unsigned rbase = 0;
  unsigned jj = 0;
  unsigned nt, half, c0s, lsa, rec[2];
  u32x2 ps_[2], pd_[2];
  u32x4 vo;
  auto issue_seg = [&](int l) {
    const unsigned nseg = min(nrec - rbase, 128u);
    nt = (nseg + 31) >> 5;
    half = (((nseg - 1) & 16u) ^ 16u) >> 4;
    const unsigned seg = lst + rbase * 4;
    rbase += nseg;
    c0s = lane_chunk(l) | 0xC00u;
    lsa = seg + (l >> 3) * 4;
    u32x4 rr;
    rr.x = lds1(lsa); rr.y = lds1(lsa + 32); rr.z = lds1(lsa + 64); rr.w = lds1(lsa + 96);
#pragma unroll
    for (int q = 0; q < 2; ++q) {
      rec[q] = lds1(seg + q * 256 + l * 4);
      if ((unsigned)(64 * q + l) >= nseg) rec[q] = SENT;
      const unsigned os = (6u * (rec[q] & 0xffffffu)) & ~3u;
      const unsigned od = rec[q] == SENT ? OOB : (6u * ((unsigned)node0 + ((rec[q] >> 24) & 31u))) & ~3u;
      asm volatile("buffer_load_dwordx2 %0, %2, %4, 0 offen\n\tbuffer_load_dwordx2 %1, %3, %4, 0 offen"
                   : "=&v"(ps_[q]), "=&v"(pd_[q]) : "v"(os), "v"(od), "s"(rpos) : "memory");
    }
    vo = rr;
  };
  auto wait_pos = [&]() {
    asm volatile("s_waitcnt vmcnt(%5)\n\ts_cmp_lg_u32 %4, 0\n\ts_cbranch_scc1 .Lw%=\n\ts_waitcnt vmcnt(0)\n.Lw%=:"
                 : "+v"(ps_[0]), "+v"(pd_[0]), "+v"(ps_[1]), "+v"(pd_[1]) : "s"(nt), "i"(4) : "scc", "memory");
  };
  u32x4 rbf;
  auto geometry = [&](int l, int q) {
        const int e = l & 31, h = l >> 5;
        const unsigned h16 = h * 16;
        const u32x2 a = norm_pos(ps_[q], rec[q]), b = norm_pos(pd_[q], (unsigned)node0 + (rec[q] >> 24));
        const unsigned r01 = pk2(lo16(a.x) - lo16(b.x), hi16(a.x) - hi16(b.x));
        const float r0 = lo16(r01), r1 = hi16(r01), r2 = rb(lo16(a.y) - lo16(b.y));
        const unsigned q01 = pk2(r0 * r0, r1 * r1);
        const float ss = rb((lo16(q01) + hi16(q01)) + rb(r2 * r2));
        const float d = rb(__builtin_amdgcn_sqrtf(rb(ss + 1e-8f)));
        const unsigned ix = min((__float_as_uint(d) >> 16) - DB0, NTAB - 1);
        if (q == 0) {
          const unsigned i0 = __builtin_amdgcn_permlane32_swap(ix, ix, false, false)[0];
          asm volatile("buffer_load_dwordx4 %0, %1, %2, 0 offen" : "=&v"(rbf) : "v"(i0 * 32 + h16), "s"(rtab) : "memory");
        }
        const float rd = __builtin_amdgcn_rcpf(d);
        const unsigned f01 = pk2(__builtin_fmaf(d, 0.0f, 1.0f), r0 * rd), f23 = pk2(r1 * rd, r2 * rd);
        const unsigned sa = scr + (2 * q + h) * 384 + (16 * (e >> 4) + 8 * ((e >> 2) & 1) + 4 * ((e >> 3) & 1) + (e & 3)) * 2;
        *(LAS unsigned short*)(size_t)sa = (unsigned short)(rec[q] == SENT ? 0xffffu : (rec[q] >> 24) & 31u);
        *(LAS unsigned short*)(size_t)(sa + 64) = (unsigned short)f01;
        *(LAS unsigned short*)(size_t)(sa + 128) = (unsigned short)(f01 >> 16);
        *(LAS unsigned short*)(size_t)(sa + 192) = (unsigned short)f23;
        *(LAS unsigned short*)(size_t)(sa + 256) = (unsigned short)(f23 >> 16);
        *(LAS unsigned short*)(size_t)(scr + (2 * q + h) * 384 + 320 + e * 2) = (unsigned short)ix;
      };
  auto issue_h = [&]() {
    dma_part<0, 0>(nt, buf, row_offsets((vo.x & 0xffffffu) << 7, (vo.y & 0xffffffu) << 7, (vo.z & 0xffffffu) << 7, (vo.w & 0xffffffu) << 7, c0s), rh);
    vo = row_offsets(vo.x << 9, vo.y << 9, vo.z << 9, vo.w << 9, c0s);
  };
  auto issue_v = [&]() {
    if (NPRE > 0) dma_part<1, 0>(nt, buf, vo, rg);
    if (NPRE > 1) dma_part<2, 0>(nt, buf, vo, rg);
  };
  auto wait_rbf = [&]() {
    asm volatile("s_waitcnt vmcnt(%2)\n\ts_cmp_lg_u32 %1, 0\n\ts_cbranch_scc1 .Lw%=\n\ts_waitcnt vmcnt(0)\n.Lw%=:"
                 : "+v"(rbf) : "s"(nt), "i"(4 * NPRE) : "scc", "memory");
  };
  unsigned ta;
  {
    const int g2 = (l >> 4) & 1, q = (l >> 2) & 3, pq = l & 3;
    ta = buf + 512 * h + 128 * q + ((4 * (pq & 1) + g2) ^ ((q & 2) | h)) * 16 + 8 * (pq >> 1);
  }
#pragma unroll
  for (int i = 0; i < 8; ++i)
    raw[i] = trread((ta ^ (unsigned)(32 * ((i >> 2) & 1) + 64 * (i & 1))) + 1024 * (i & 3));
  issue_seg(l);
  issue_h();
  wait_pos();
  geometry(l, 0);
  geometry(l, 1);
  asm volatile("s_barrier" ::: "memory");
#pragma unroll
  for (int i = 8; i < 32; ++i)
    raw[i] = trread((ta ^ (unsigned)(32 * ((i >> 2) & 1) + 64 * (i & 1))) + 4096 * (i >> 3) + 1024 * (i & 3));
  issue_v();
  f32x16 acc[8];
  {
#pragma unroll
    for (int i = 0; i < 8; ++i) {
      const u32x4 f0 = {raw[4 * i].x, raw[4 * i].y, raw[4 * i + 1].x, raw[4 * i + 1].y};
      const u32x4 f1 = {raw[4 * i + 2].x, raw[4 * i + 2].y, raw[4 * i + 3].x, raw[4 * i + 3].y};
      const f32x16 z = {};
      acc[i] = MFMA(__builtin_bit_cast(bf16x8, f1), id[1], MFMA(__builtin_bit_cast(bf16x8, f0), id[0], z));
    }
  }

  wait_rbf();
  asm volatile("s_barrier" ::: "memory");
  for (;;) {
    int l;
    asm volatile("v_mbcnt_lo_u32_b32 %0, -1, 0\n\tv_mbcnt_hi_u32_b32 %0, -1, %0" : "=v"(l));
    const int e = l & 31, h = l >> 5;
    if (nt > 0) {
      unsigned tix = scr + 320 + e * 2, h16 = h * 16;
      unsigned tra;
      {
        const int g2 = (l >> 4) & 1, q = (l >> 2) & 3, pq = l & 3;
        tra = buf + 4096 + 512 * h + 128 * (q >> 1) + ((4 * (pq & 1) + g2) ^ h) * 16 + 8 * (pq >> 1);
        if (!(q & 1)) tra = 0x40000u;
      }
      unsigned rowa = buf + e * 128 + ((4 * h) ^ swz(e)) * 16;
      unsigned nodepk = (unsigned)e * 0x00010001u, sch = scr + h16, wl = lb + L_W + l * 16;
      unsigned ntc = __builtin_amdgcn_readfirstlane(nt);
      const unsigned halfc = __builtin_amdgcn_readfirstlane(half);
      asm volatile(PRE_WAIT TILE_LOOP_ASM
                   : "+{v[128:143]}"(acc[0]), "+{v[144:159]}"(acc[1]), "+{v[160:175]}"(acc[2]), "+{v[176:191]}"(acc[3]),
                     "+{v[192:207]}"(acc[4]), "+{v[208:223]}"(acc[5]), "+{v[224:239]}"(acc[6]), "+{v[240:255]}"(acc[7]),
                     "+{v5}"(sch), "+{v6}"(lsa), "+{v7}"(tix), "+{v[12:15]}"(rbf), "+{v[112:115]}"(vo), [nt] "+s"(ntc)
                   : "{v0}"(c0s), "{v1}"(rowa), "{v2}"(wl), "{v3}"(tra), "{v4}"(nodepk), "{v8}"(h16),
                     [buf] "s"(buf), [half] "s"(halfc), [rh] "s"(rh), [rg] "s"(rg), [rtab] "s"(rtab)
                   : "v9", "v10", "v11", "v16", "v17", "v18", "v19", CL10(2), CL10(3), CL10(4), CL10(5), CL10(6), CL10(7),
                     CL10(8), CL10(9), CL10(10), "v110", "v111", "v116", "v117", "v118", "v119", CL8(12), "m0", "scc", "vcc", "memory");
    }
    PIN("=v"(rec[0]), "=v"(rec[1]), "=v"(c0s), "=v"(lsa), "=v"(vo), "=v"(ps_[0]), "=v"(ps_[1]), "=v"(pd_[0]), "=v"(pd_[1]), "=v"(rbf));
    if (rbase >= nrec && grp >= 4) break;
    asm volatile("v_mbcnt_lo_u32_b32 %0, -1, 0\n\tv_mbcnt_hi_u32_b32 %0, -1, %0" : "=v"(l));
    if (rbase >= nrec) {
      const __amdgpu_buffer_rsrc_t rw = __builtin_amdgcn_make_buffer_rsrc((void*)ws, (short)0, (int)WS_H, 0x00020000);
      unsigned n = 0;
      while (grp < 4 && n <= 160u) {
        const unsigned slot = grp * 64 + l, sq = slot / PSL, q = slot < (unsigned)MAXP ? sq + 8 * (slot - PSL * sq) : ~0u;
        const unsigned co = (unsigned)WS_CELLA + ((unsigned)bin * MAXP + slot) * 32;
        const unsigned hd = __builtin_amdgcn_raw_buffer_load_b32(rw, co + 28, 0, 0);
        const unsigned c = q < (unsigned)P ? ((hd >= 0xE0000000u && hd != SENT) ? hd & 0x1fffu : 8u) : 0u;
        while (n <= 160u) {
          if (!__any(jj < c)) { ++grp; jj = 0; break; }
          const unsigned xo = c > 15 ? (unsigned)WS_SORT + (q * EPW + ((hd >> 13) & 0x1fffu) + jj) * 4
                            : (c > 8 && jj >= 7) ? co + (unsigned)(WS_CELLB - WS_CELLA) + (jj - 7) * 4 : co + jj * 4;
          const unsigned x = __builtin_amdgcn_raw_buffer_load_b32(rw, xo, 0, 0);
          const bool ok = jj < c && (x >> 29) == (unsigned)win;
          const unsigned long long mk = __ballot(ok);
          const unsigned ps = n + __builtin_amdgcn_mbcnt_hi((unsigned)(mk >> 32), __builtin_amdgcn_mbcnt_lo((unsigned)mk, 0u));
          if (ok) sts1(lst + ps * 4, x);
          n += __builtin_popcountll(mk);
          ++jj;
        }
      }
      if (l < 32) sts1(lst + (n + l) * 4, SENT);
      nrec = n;
      rbase = 0;
      wave_sync();
    }
    issue_seg(l);
    issue_h();
    wait_pos();
    geometry(l, 0);
    issue_v();
    geometry(l, 1);
    wait_rbf();
  }

  {
    int l2;
    asm volatile("v_mbcnt_lo_u32_b32 %0, -1, 0\n\tv_mbcnt_hi_u32_b32 %0, -1, %0" : "=v"(l2));
    const unsigned rowa = buf + (l2 & 31) * 128 + ((4 * (l2 >> 5)) ^ swz(l2 & 31)) * 16;
    const __amdgpu_buffer_rsrc_t rout = __builtin_amdgcn_make_buffer_rsrc((void*)out, (short)0, N * 512, 0x00020000);
    const unsigned c2 = lane_chunk(l2), r0 = (unsigned)(node0 + (l2 >> 3)) * 512u;
    const unsigned oo[4] = {r0 + c2, r0 + 4096 + (c2 ^ 64), r0 + 8192 + c2, l2 < 32 ? r0 + 12288 + (c2 ^ 64) : OOB};
    u32x4 x[4];
#pragma unroll
    for (int part = 0; part <= 4; ++part) {
      if (part < 4) {
#pragma unroll
        for (int q = 0; q < 4; ++q) sts4((rowa ^ (q * 16)) + part * 4096, pack8u(acc[part * 2 + (q >> 1)], q & 1));
      }
      if (part > 0) {
#pragma unroll
        for (int i = 0; i < 4; ++i) __builtin_amdgcn_raw_buffer_store_b128(x[i], rout, oo[i] + (part - 1) * 128, 0, O_AUX);
      }
      wave_sync();
      if (part < 4) {
#pragma unroll
        for (int i = 0; i < 4; ++i) x[i] = lds4(buf + part * 4096 + i * 1024 + l2 * 16);
      }
      SB();
    }
  }
  asm volatile("painn_code_end:");
}

__global__ void fb_phi(const uint16_t* sv, const uint16_t* w1, const uint16_t* w2, float* phi, float* acc) {
  __shared__ float s[64], hh[64];
  const size_t n = blockIdx.x; const int t = threadIdx.x;
  if (t < 64) s[t] = lo16(sv[n * 256 + t]);
  __syncthreads();
  if (t < 64) { float a = 0; for (int i = 0; i < 64; ++i) a += s[i] * lo16(w1[i * 64 + t]); a = rb(a); hh[t] = rb(a / (1.f + __expf(-a))); }
  __syncthreads();
  float a = 0;
  for (int j = 0; j < 64; ++j) a += hh[j] * lo16(w2[j * 192 + t]);
  phi[n * 192 + t] = rb(a);
  acc[n * 256 + t] = 0.f;
  if (t < 64) acc[n * 256 + 192 + t] = 0.f;
}
__global__ void fb_edge(const uint16_t* sv, const uint16_t* pos, const int* src, const int* dst, const uint16_t* freqs,
                        const uint16_t* wr, const float* phi, float* acc, int E) {
  const int e = blockIdx.x * 4 + (threadIdx.x >> 6), t = threadIdx.x & 63;
  if (e >= E) return;
  const size_t a = src[e], b = dst[e];
  const float r0 = rb(lo16(pos[a * 3]) - lo16(pos[b * 3])), r1 = rb(lo16(pos[a * 3 + 1]) - lo16(pos[b * 3 + 1])),
              r2 = rb(lo16(pos[a * 3 + 2]) - lo16(pos[b * 3 + 2]));
  const float d = rb(__builtin_amdgcn_sqrtf(rb(rb((rb(r0 * r0) + rb(r1 * r1)) + rb(r2 * r2)) + 1e-8f)));
  float fc = rb(0.5f * rb(rb(__builtin_amdgcn_cosf(rb(rb(3.14159265358979f * d) * 0.2f) * 0.15915494309189535f)) + 1.0f));
  fc = d < 5.0f ? fc : 0.0f * d;
  if (fc == 0.0f) return;
  const float rd = __builtin_amdgcn_rcpf(d);
  float W0 = 0, W1 = 0, W2 = 0;
  for (int k = 0; k < 16; ++k) {
    const float x = rb(d * lo16(freqs[k]));
    const float q = rb(rb(__builtin_amdgcn_sinf(__builtin_fmaf(x, -1.8562245109e-06f, __builtin_amdgcn_fractf(x * 0.15915679931640625f)))) * rd);
    W0 += q * lo16(wr[k * 192 + t]); W1 += q * lo16(wr[k * 192 + 64 + t]); W2 += q * lo16(wr[k * 192 + 128 + t]);
  }
  const float m0 = rb(phi[a * 192 + t] * rb(rb(W0) * fc)), m1 = rb(phi[a * 192 + 64 + t] * rb(rb(W1) * fc)),
              m2 = rb(phi[a * 192 + 128 + t] * rb(rb(W2) * fc));
  atomicAdd(&acc[b * 256 + t], m0);
  atomicAdd(&acc[b * 256 + 64 + t], rb(rb(m1 * lo16(sv[a * 256 + 64 + t])) + rb(m2 * rb(r0 * rd))));
  atomicAdd(&acc[b * 256 + 128 + t], rb(rb(m1 * lo16(sv[a * 256 + 128 + t])) + rb(m2 * rb(r1 * rd))));
  atomicAdd(&acc[b * 256 + 192 + t], rb(rb(m1 * lo16(sv[a * 256 + 192 + t])) + rb(m2 * rb(r2 * rd))));
}
__global__ void fb_out(const uint16_t* sv, const float* acc, uint16_t* out, int n) {
  const int i = blockIdx.x * 256 + threadIdx.x;
  if (i < n) out[i] = (uint16_t)pk2(lo16(sv[i]) + rb(acc[i]), 0.0f);
}

extern "C" void kernel_launch(void* const* d_in, const int* in_sizes, int n_in,
                              void* d_out, int out_size, void* d_ws, size_t ws_size,
                              hipStream_t stream) {
  const int N = in_sizes[1] / 3, E = in_sizes[2];
  const int P = (E + EPW - 1) / EPW, NB = (N + NPB - 1) / NPB;
  if (P > MAXP || N > MAXN || ws_size < WS_H + (size_t)N * 128) {
    if (ws_size < (size_t)N * 448 * 4) return;
    float* phi = (float*)d_ws; float* acc = phi + (size_t)N * 192;
    fb_phi<<<N, 192, 0, stream>>>((const uint16_t*)d_in[0], (const uint16_t*)d_in[5], (const uint16_t*)d_in[6], phi, acc);
    fb_edge<<<(E + 3) / 4, 256, 0, stream>>>((const uint16_t*)d_in[0], (const uint16_t*)d_in[1], (const int*)d_in[2], (const int*)d_in[3],
                                             (const uint16_t*)d_in[4], (const uint16_t*)d_in[7], phi, acc, E);
    fb_out<<<(out_size + 255) / 256, 256, 0, stream>>>((const uint16_t*)d_in[0], acc, (uint16_t*)d_out, out_size);
    return;
  }
  (void)hipFuncSetAttribute((const void*)bin_edges, hipFuncAttributeMaxDynamicSharedMemorySize, T_END);
  (void)hipFuncSetAttribute((const void*)node_update, hipFuncAttributeMaxDynamicSharedMemorySize, L_END);
  bin_edges<<<GRID, K1T, T_END, stream>>>((const char*)d_in[0], (const char*)d_in[1], (const int*)d_in[2], (const int*)d_in[3],
                                          (const uint16_t*)d_in[4], (const char*)d_in[5], (const uint16_t*)d_in[6],
                                          (const uint16_t*)d_in[7], (char*)d_ws, N, E, P, NB);
  node_update<<<NB, K2T, L_END, stream>>>((const char*)d_in[0], (const char*)d_in[1], (const char*)d_ws,
                                          (char*)d_out, N, P);
}
```
